```python
import torch

def _softmax(x, axis=-1):
    return torch.softmax(x, dim=axis)

def _gelu(x):
    return torch.nn.functional.gelu(x, approximate='tanh')

def _silu(x):
    return torch.nn.functional.silu(x)

def _sigmoid(x):
    return torch.sigmoid(x)

def _layernorm(x, axis=-1, eps=1e-05):
    m = x.mean(dim=axis, keepdim=True)
    v = x.var(dim=axis, keepdim=True, unbiased=False)
    return (x - m) / torch.sqrt(v + eps)

def _scatter_add(tgt, idx, src, axis=0):
    return tgt.clone().index_add_(axis, idx, src)

def _segment_sum(src, seg, n):
    out = torch.zeros((n,) + src.shape[1:], dtype=src.dtype)
    return out.index_add_(0, seg, src)

def _pad(x, pw):
    flat = tuple((v for pair in reversed(pw) for v in pair))
    return torch.nn.functional.pad(x, flat)
kernel_name = 'adaLN_cond__linear_kernel_hedgehog__glu_gated_geglu__prompt'

def setup_inputs():

    _w = lambda a: (lambda t: t.to(torch.bfloat16) if t.is_floating_point() else t)(torch.as_tensor(a))

    def _A_setup():
        B, S, D, H = (1, 2048, 576, 32)
        Dh = D // H
        return {'x': _w((lambda *s: torch.randn(*s, dtype=torch.bfloat16) * 0.05)(B, S, D)), 'wqkv': _w((lambda *s: torch.randn(*s, dtype=torch.bfloat16) * 0.4)(D, 3 * D)), 'w_phi': _w((lambda *s: torch.randn(*s, dtype=torch.bfloat16) * 0.4)(H, Dh, Dh) * Dh ** (-0.5)), 'wo': _w((lambda *s: torch.randn(*s, dtype=torch.bfloat16) * 0.05)(D, D))}
    _A_in = _A_setup()

    def _B_setup():
        B, S, Dm, Df = (1, 2048, 576, 4608)
        return {'x': _w((lambda *s: torch.randn(*s, dtype=torch.bfloat16) * 0.05)(B, S, Dm)), 'w_gate': _w((lambda *s: torch.randn(*s, dtype=torch.bfloat16) * 1.6)(Dm, Df) * Dm ** (-0.5)), 'w_up': _w((lambda *s: torch.randn(*s, dtype=torch.bfloat16) * 1.6)(Dm, Df) * Dm ** (-0.5)), 'w_down': _w((lambda *s: torch.randn(*s, dtype=torch.bfloat16) * 1.6)(Df, Dm) * Df ** (-0.5))}
    _B_in = _B_setup()
    return {'A_x': _A_in['x'], 'A_wqkv': _A_in['wqkv'], 'A_w_phi': _A_in['w_phi'], 'A_wo': _A_in['wo'], 'B_x': _B_in['x'], 'B_w_gate': _B_in['w_gate'], 'B_w_up': _B_in['w_up'], 'B_w_down': _B_in['w_down'], 'c': _w((lambda *s: torch.randn(*s, dtype=torch.bfloat16) * 0.1)(1, 576) * 8.0), 'w_mod': _w((lambda *s: torch.randn(*s, dtype=torch.bfloat16) * 0.1)(576, 6 * 576) * 576 ** (-0.5) * 8.0)}

def reference(A_x, A_wqkv, A_w_phi, A_wo, B_x, B_w_gate, B_w_up, B_w_down, c, w_mod):

    def _A_ref(x, wqkv, w_phi, wo):
        B, S, D = x.shape
        H = 32
        Dh = D // H
        qkv = (lambda x, *d: x.permute(*d))((x @ wqkv).reshape(B, S, 3, H, Dh), 2, 0, 3, 1, 4)
        q, k, v = (qkv[0], qkv[1], qkv[2])
        pq = torch.einsum('bhsd,hdf->bhsf', q, w_phi)
        pk = torch.einsum('bhsd,hdf->bhsf', k, w_phi)
        phi_q = torch.concatenate([_softmax(pq, axis=-1), _softmax(-pq, axis=-1)], axis=-1)
        phi_k = torch.concatenate([_softmax(pk, axis=-1), _softmax(-pk, axis=-1)], axis=-1)
        kv = torch.einsum('bhsf,bhsd->bhfd', phi_k, v)
        z = torch.einsum('bhsf->bhf', phi_k)
        num = torch.einsum('bhqf,bhfd->bhqd', phi_q, kv)
        den = torch.einsum('bhqf,bhf->bhq', phi_q, z)[..., None] + 1e-06
        o = num / den
        return (lambda x, *d: x.permute(*d))(o, 0, 2, 1, 3).reshape(B, S, D) @ wo

    def _B_ref(x, w_gate, w_up, w_down):
        g = torch.einsum('bsd,df->bsf', x, w_gate)
        u = torch.einsum('bsd,df->bsf', x, w_up)
        h = _gelu(g) * u
        return torch.einsum('bsf,fd->bsd', h, w_down)
    x = A_x
    mod = (_silu(c) @ w_mod).reshape(c.shape[0], 6, 1, -1)
    sa, ca, ga, sb, cb, gb = (mod[:, 0].clone(), mod[:, 1].clone(), mod[:, 2].clone(), mod[:, 3].clone(), mod[:, 4].clone(), mod[:, 5].clone())
    h = _layernorm(x, axis=-1) * (1 + ca) + sa
    x = x + ga * _A_ref(h, A_wqkv, A_w_phi, A_wo)
    h = _layernorm(x, axis=-1) * (1 + cb) + sb
    x = x + gb * _B_ref(h, B_w_gate, B_w_up, B_w_down)
    return x
```

```cpp
#include <hip/hip_runtime.h>
#include <hip/hip_bf16.h>
#include <stdint.h>

typedef short bf16x8 __attribute__((ext_vector_type(8)));
typedef short bf16x4 __attribute__((ext_vector_type(4)));
typedef float f32x4 __attribute__((ext_vector_type(4)));
typedef float f32x2 __attribute__((ext_vector_type(2)));
typedef unsigned u32x4 __attribute__((ext_vector_type(4)));
typedef unsigned u32x2 __attribute__((ext_vector_type(2)));
typedef __bf16 bfv2 __attribute__((ext_vector_type(2)));
#define GAS __attribute__((address_space(1)))
#define LAS __attribute__((address_space(3)))
typedef GAS unsigned gu32;
typedef GAS unsigned long long gu64;
#define RLX_AGENT __ATOMIC_RELAXED, __HIP_MEMORY_SCOPE_AGENT

constexpr int S_ = 2048, D_ = 576, DF_ = 4608;

constexpr size_t WS_FLAGS = 0;
constexpr size_t WS_FLAGS_BYTES = 196608;
constexpr size_t OFF_CNT1 = 0;
constexpr size_t OFF_CNT4 = 4096;
constexpr size_t OFF_GRAN2 = 65536;
constexpr size_t WS_MODF = WS_FLAGS + WS_FLAGS_BYTES;
constexpr size_t WS_MODH = WS_MODF + 16384;
constexpr size_t WS_XN = WS_MODH + 8192;
constexpr size_t FRAG_ACT = 128 * 18 * 1024;
constexpr size_t WS_WQ = WS_XN + FRAG_ACT;
constexpr size_t WS_WO = WS_WQ + 16 * 126 * 1024;
constexpr size_t WS_WPHI = WS_WO + 36 * 18 * 1024;
constexpr size_t WS_KVP = WS_WPHI + 16 * 8 * 1024;
constexpr size_t WS_O = WS_KVP + 16 * 16 * 12 * 1024;
constexpr size_t WS_X1 = WS_O + FRAG_ACT;
constexpr size_t WS_H2 = WS_X1 + FRAG_ACT;
constexpr size_t WS_HH = WS_H2 + FRAG_ACT;
constexpr size_t WS_SLAB = WS_HH + (size_t)128 * 144 * 1024;
constexpr size_t WS_END = WS_SLAB + (size_t)64 * 4 * 8 * 9 * 1024;

#define ABL 0
#define STAMP_DECL
#define STAMP(kid, i)
#define STAMPR(kid, i)
__device__ __forceinline__ float bf_lo(unsigned u) { return __uint_as_float(u << 16); }
__device__ __forceinline__ float bf_hi(unsigned u) { return __uint_as_float(u & 0xffff0000u); }
__device__ __forceinline__ float bf2f(unsigned short u) { return __uint_as_float(((unsigned)u) << 16); }
__device__ __forceinline__ unsigned pack2(float a, float b) {
  f32x2 v = {a, b};
  bfv2 r = __builtin_convertvector(v, bfv2);
  return __builtin_bit_cast(unsigned, r);
}
__device__ __forceinline__ float rbf(float a) { return (float)(__bf16)a; }
__device__ __forceinline__ f32x4 mfma16(bf16x8 a, bf16x8 b, f32x4 c) {
  return __builtin_amdgcn_mfma_f32_16x16x32_bf16(a, b, c, 0, 0, 0);
}
__device__ __forceinline__ f32x4 mfmaL(bf16x8 a, bf16x8 b, f32x4 c) {
  return __builtin_amdgcn_mfma_f32_16x16x32_bf16(a, b, c, 0, 0, 0);
}
__device__ __forceinline__ bf16x8 mk8(unsigned a, unsigned b, unsigned c, unsigned d) {
  u32x4 v = {a, b, c, d};
  return __builtin_bit_cast(bf16x8, v);
}
__device__ __forceinline__ bf16x4 tr_read(const char* p) {
  return __builtin_amdgcn_ds_read_tr16_b64_v4i16((LAS bf16x4*)p);
}
__device__ __forceinline__ bf16x8 cat44(bf16x4 a, bf16x4 b) {
  return __builtin_shufflevector(a, b, 0, 1, 2, 3, 4, 5, 6, 7);
}
__device__ __forceinline__ void glds16(const void* g, void* l) {
  __builtin_amdgcn_global_load_lds((const GAS unsigned*)g, (LAS unsigned*)l, 16, 0, 0);
}
__device__ __forceinline__ __amdgpu_buffer_rsrc_t mk_rsrc(const void* base, unsigned bytes) {
  return __builtin_amdgcn_make_buffer_rsrc((void*)base, 0, bytes, 0x00020000);
}
__device__ __forceinline__ u32x4 mk_desc(const void* base, unsigned bytes) {
  const unsigned long long a = (unsigned long long)base;
  return (u32x4){(unsigned)a, (unsigned)(a >> 32) & 0xffffu, bytes, 0x00020000u};
}
__device__ __forceinline__ void blds16(u32x4 desc, unsigned lds_addr, int voff, int soff) {
  unsigned keep;
  asm volatile("s_mov_b32 %0, m0\n\ts_mov_b32 m0, %2\n\ts_nop 0\n\tbuffer_load_dwordx4 %1, %3, %4 offen lds\n\ts_mov_b32 m0, %0"
               : "=&s"(keep) : "v"(voff), "s"(lds_addr), "s"(desc), "s"(soff) : "memory");
}
__device__ __forceinline__ u32x4 bload16(u32x4 desc, int voff, int soff) {
  u32x4 r;
  asm volatile("buffer_load_dwordx4 %0, %1, %2, %3 offen" : "=v"(r) : "v"(voff), "s"(desc), "s"(soff) : "memory");
  return r;
}
__device__ __forceinline__ unsigned lds_addr_of(const void* p) { return (unsigned)(unsigned long long)(LAS const char*)p; }
__device__ __forceinline__ float gmax(float v) {
  auto r = __builtin_amdgcn_permlane16_swap(__float_as_uint(v), __float_as_uint(v), false, false);
  v = fmaxf(__uint_as_float(r[0]), __uint_as_float(r[1]));
  auto q = __builtin_amdgcn_permlane32_swap(__float_as_uint(v), __float_as_uint(v), false, false);
  return fmaxf(__uint_as_float(q[0]), __uint_as_float(q[1]));
}
__device__ __forceinline__ float gmin(float v) {
  auto r = __builtin_amdgcn_permlane16_swap(__float_as_uint(v), __float_as_uint(v), false, false);
  v = fminf(__uint_as_float(r[0]), __uint_as_float(r[1]));
  auto q = __builtin_amdgcn_permlane32_swap(__float_as_uint(v), __float_as_uint(v), false, false);
  return fminf(__uint_as_float(q[0]), __uint_as_float(q[1]));
}
__device__ __forceinline__ float gsum(float v) {
  auto r = __builtin_amdgcn_permlane16_swap(__float_as_uint(v), __float_as_uint(v), false, false);
  v = __uint_as_float(r[0]) + __uint_as_float(r[1]);
  auto q = __builtin_amdgcn_permlane32_swap(__float_as_uint(v), __float_as_uint(v), false, false);
  return __uint_as_float(q[0]) + __uint_as_float(q[1]);
}
#define WAIT_V(n) asm volatile("s_waitcnt vmcnt(" #n ")" ::: "memory")
#define WAIT_L0() asm volatile("s_waitcnt lgkmcnt(0)" ::: "memory")
#define RAW_BAR() do { asm volatile("s_waitcnt lgkmcnt(0)" ::: "memory"); __builtin_amdgcn_s_barrier(); } while (0)
constexpr unsigned kSpinCap = 1u << 20;

__device__ __forceinline__ void wait_ge(gu32* word, unsigned want) {
  for (unsigned spins = 0; spins < kSpinCap; ++spins) {
    unsigned v = (unsigned)__builtin_amdgcn_readfirstlane(__hip_atomic_load(word, RLX_AGENT));
    if (v >= want) return;
    __builtin_amdgcn_s_sleep(1);
  }
}

constexpr int K0_GEMV = 108, K0_LN = 128, K0_RQ = 126, K0_RO = 41, K0_RP = 8, K0_Z = 12;
constexpr int K0_GRID = K0_GEMV + K0_LN + K0_RQ + K0_RO + K0_RP + K0_Z;

__global__ __launch_bounds__(256) void k0_prep(const unsigned short* __restrict__ x, const unsigned short* __restrict__ wqkv,
                                               const unsigned short* __restrict__ wphi, const unsigned short* __restrict__ wo,
                                               const unsigned short* __restrict__ c, const unsigned short* __restrict__ wmod,
                                               char* __restrict__ ws) {
  __shared__ float red[64 * 33 + 8 * 32];
  const int t = threadIdx.x;
  int b = blockIdx.x;
  if (b < K0_GEMV) {
    const int cc = t & 3, rg = t >> 2;
    u32x4 wv[9];
    unsigned short cv[9];
#pragma unroll
    for (int i = 0; i < 9; ++i) wv[i] = *(const u32x4*)(wmod + (size_t)(rg + 64 * i) * 3456 + b * 32 + cc * 8);
#pragma unroll
    for (int i = 0; i < 9; ++i) cv[i] = c[rg + 64 * i];
    float acc[8] = {0, 0, 0, 0, 0, 0, 0, 0};
#pragma unroll
    for (int i = 0; i < 9; ++i) {
      float cf = bf2f(cv[i]);
      float s = rbf(cf / (1.0f + __expf(-cf)));
#pragma unroll
      for (int j = 0; j < 4; ++j) {
        acc[2 * j] += s * bf_lo(wv[i][j]);
        acc[2 * j + 1] += s * bf_hi(wv[i][j]);
      }
    }
#pragma unroll
    for (int j = 0; j < 8; ++j) red[rg * 33 + cc * 8 + j] = acc[j];
    __syncthreads();
    {
      const int col = t & 31, part = t >> 5;
      float s = 0;
#pragma unroll
      for (int i = 0; i < 8; ++i) s += red[(part * 8 + i) * 33 + col];
      red[64 * 33 + part * 32 + col] = s;
    }
    __syncthreads();
    if (t < 32) {
      float s = 0;
#pragma unroll
      for (int i = 0; i < 8; ++i) s += red[64 * 33 + i * 32 + t];
      const int idx = b * 32 + t, sec = idx / 576;
      float v = rbf(s);
      if (sec == 1 || sec == 4) v = rbf(1.0f + v);
      ((float*)(ws + WS_MODF))[idx] = v;
      ((unsigned short*)(ws + WS_MODH))[idx] = (unsigned short)(__float_as_uint(v) >> 16);
    }
    return;
  }
  b -= K0_GEMV;
  if (b < K0_LN) {
    const int tok = t & 15, g = (t >> 4) & 3, w = t >> 6;
    u32x4 xv[5];
    const unsigned short* xr = x + (size_t)(b * 16 + tok) * 576 + g * 8;
#pragma unroll
    for (int i = 0; i < 5; ++i) {
      int ks = w + 4 * i;
      if (ks < 18) xv[i] = *(const u32x4*)(xr + ks * 32);
      else xv[i] = (u32x4){0, 0, 0, 0};
    }
    float s = 0;
#pragma unroll
    for (int i = 0; i < 5; ++i)
#pragma unroll
      for (int j = 0; j < 4; ++j) s += bf_lo(xv[i][j]) + bf_hi(xv[i][j]);
    s = gsum(s);
    if (g == 0) red[w * 16 + tok] = s;
    __syncthreads();
    const float mean = (red[tok] + red[16 + tok] + red[32 + tok] + red[48 + tok]) * (1.0f / 576.0f);
    float q = 0;
#pragma unroll
    for (int i = 0; i < 5; ++i) {
      if (w + 4 * i < 18) {
#pragma unroll
        for (int j = 0; j < 4; ++j) {
          float a = bf_lo(xv[i][j]) - mean, bb = bf_hi(xv[i][j]) - mean;
          q += a * a + bb * bb;
        }
      }
    }
    q = gsum(q);
    if (g == 0) red[64 + w * 16 + tok] = q;
    __syncthreads();
    const float var = (red[64 + tok] + red[80 + tok] + red[96 + tok] + red[112 + tok]) * (1.0f / 576.0f);
    const float mb = rbf(mean);
    const float sd = rbf(sqrtf(rbf(rbf(var) + 1e-5f)));
    const float rs = 1.0f / sd;
#pragma unroll
    for (int i = 0; i < 5; ++i) {
      int ks = w + 4 * i;
      if (ks < 18) {
        u32x4 o;
#pragma unroll
        for (int j = 0; j < 4; ++j)
          o[j] = pack2(rbf(bf_lo(xv[i][j]) - mb) * rs, rbf(bf_hi(xv[i][j]) - mb) * rs);
        *(u32x4*)(ws + WS_XN + ((size_t)(b * 18 + ks) * 64 + (t & 63)) * 16) = o;
      }
    }
    return;
  }
  b -= K0_LN;
  if (b < K0_RQ) {
#pragma unroll
    for (int r = 0; r < 4; ++r) {
      const int id = b * 1024 + r * 256 + t;
      const int lane = id & 63, f = id >> 6;
      const int nt = f % 7, ks = (f / 7) % 18, p = f / 126;
      const int cidx = nt * 16 + (lane & 15), g = lane >> 4;
      int col = -1;
      if (cidx < 18) col = 1152 + 36 * p + cidx;
      else if (cidx < 20) col = -1;
      else if (cidx < 38) col = 1152 + 36 * p + 18 + (cidx - 20);
      else if (cidx < 74) col = 36 * p + (cidx - 38);
      else if (cidx < 110) col = 576 + 36 * p + (cidx - 74);
      unsigned short v[8];
#pragma unroll
      for (int j = 0; j < 8; ++j) v[j] = (col >= 0) ? wqkv[(size_t)(ks * 32 + g * 8 + j) * 1728 + col] : (unsigned short)0;
      u32x4 o = {v[0] | ((unsigned)v[1] << 16), v[2] | ((unsigned)v[3] << 16), v[4] | ((unsigned)v[5] << 16), v[6] | ((unsigned)v[7] << 16)};
      *(u32x4*)(ws + WS_WQ + (size_t)id * 16) = o;
    }
    return;
  }
  b -= K0_RQ;
  if (b < K0_RO) {
#pragma unroll
    for (int r = 0; r < 4; ++r) {
      const int id = b * 1024 + r * 256 + t;
      if (id < 41472) {
        const int lane = id & 63, f = id >> 6;
        const int ks = f % 18, nt = f / 18;
        const int col = nt * 16 + (lane & 15), g = lane >> 4;
        unsigned short v[8];
#pragma unroll
        for (int j = 0; j < 8; ++j) v[j] = wo[(size_t)(ks * 32 + g * 8 + j) * 576 + col];
        u32x4 o = {v[0] | ((unsigned)v[1] << 16), v[2] | ((unsigned)v[3] << 16), v[4] | ((unsigned)v[5] << 16), v[6] | ((unsigned)v[7] << 16)};
        *(u32x4*)(ws + WS_WO + (size_t)id * 16) = o;
      }
    }
    return;
  }
  b -= K0_RO;
  if (b < K0_RP) {
#pragma unroll
    for (int r = 0; r < 4; ++r) {
      const int id = b * 1024 + r * 256 + t;
      const int lane = id & 63, f8 = id >> 6;
      const int ft = f8 & 1, seg = (f8 >> 1) & 3, p = f8 >> 3;
      const int s0 = 38 + 18 * seg, nt0 = s0 >> 4, h = 2 * p + (seg & 1);
      const int f = ft * 16 + (lane & 15), g = lane >> 4;
      unsigned short v[8];
#pragma unroll
      for (int j = 0; j < 8; ++j) {
        const int col = 16 * (nt0 + (j >> 2)) + 4 * g + (j & 3), d = col - s0;
        v[j] = (d >= 0 && d < 18 && f < 18) ? wphi[(h * 18 + d) * 18 + f] : (unsigned short)0;
      }
      u32x4 o = {v[0] | ((unsigned)v[1] << 16), v[2] | ((unsigned)v[3] << 16), v[4] | ((unsigned)v[5] << 16), v[6] | ((unsigned)v[7] << 16)};
      *(u32x4*)(ws + WS_WPHI + (size_t)id * 16) = o;
    }
    return;
  }
  b -= K0_RP;
  {
    u32x4 z = {0, 0, 0, 0};
#pragma unroll
    for (int r = 0; r < 4; ++r) *(u32x4*)(ws + WS_FLAGS + (size_t)b * 16384 + r * 4096 + t * 16) = z;
  }
}

constexpr int K1_LDS_B = 0, K1_LDS_PHI = 129024, K1_LDS_V = 147456, K1_LDS_MISC = 159744, K1_LDS = 159744 + 64;

struct Feat { unsigned pos01, pos23, neg01, neg23, ex0, ex1; };

__device__ __forceinline__ Feat feature_map(bf16x8 w0, bf16x8 w1, bf16x8 bop, int g) {
  f32x4 z = {0, 0, 0, 0};
  f32x4 t0 = mfma16(w0, bop, z);
  f32x4 t1 = mfma16(w1, bop, z);
  float p0 = rbf(t0[0]), p1 = rbf(t0[1]), p2 = rbf(t0[2]), p3 = rbf(t0[3]);
  float e0 = rbf(t1[0]), e1 = rbf(t1[1]);
  const bool g0 = (g == 0);
  if (!g0) { e0 = p0; e1 = p1; }
  float mx = fmaxf(fmaxf(fmaxf(p0, p1), fmaxf(p2, p3)), fmaxf(e0, e1));
  float mn = fminf(fminf(fminf(p0, p1), fminf(p2, p3)), fminf(e0, e1));
  mx = gmax(mx);
  mn = gmin(mn);
  float a0 = __expf(p0 - mx), a1 = __expf(p1 - mx), a2 = __expf(p2 - mx), a3 = __expf(p3 - mx);
  float b0 = __expf(mn - p0), b1 = __expf(mn - p1), b2 = __expf(mn - p2), b3 = __expf(mn - p3);
  float ae0 = __expf(e0 - mx), ae1 = __expf(e1 - mx), be0 = __expf(mn - e0), be1 = __expf(mn - e1);
  if (!g0) { ae0 = 0; ae1 = 0; be0 = 0; be1 = 0; }
  float sp = gsum((a0 + a1) + (a2 + a3) + (ae0 + ae1));
  float sn = gsum((b0 + b1) + (b2 + b3) + (be0 + be1));
  float rp = __builtin_amdgcn_rcpf(sp), rn = __builtin_amdgcn_rcpf(sn);
  Feat f;
  f.pos01 = pack2(a0 * rp, a1 * rp);
  f.pos23 = pack2(a2 * rp, a3 * rp);
  f.neg01 = pack2(b0 * rn, b1 * rn);
  f.neg23 = pack2(b2 * rn, b3 * rn);
  f.ex0 = pack2(ae0 * rp, ae1 * rp);
  f.ex1 = pack2(be0 * rn, be1 * rn);
  return f;
}

__global__ __launch_bounds__(512, 2) __attribute__((amdgpu_waves_per_eu(2, 2))) void k1_attn(char* __restrict__ ws) {
  extern __shared__ __attribute__((aligned(16))) char smem[];
  const int b = blockIdx.x, xcd = b & 7, bi = b >> 3;
  const int T = (xcd >> 1) * 4 + (bi & 3), p = (xcd & 1) * 8 + (bi >> 2);
  const int w = __builtin_amdgcn_readfirstlane(threadIdx.x >> 6), l = threadIdx.x & 63;
  const int tok = l & 15, g = l >> 4;
  if (threadIdx.x == 0) *(LAS unsigned*)(smem + K1_LDS_MISC) = 0;
  STAMP_DECL; STAMPR(1, 14); STAMP(1, 0);

  const int mt = T * 8 + w;
  const u32x4 dW = mk_desc(ws, 0x7fffffffu);
  u32x4 mv = bload16(dW, (int)WS_MODF + (threadIdx.x < 288 ? threadIdx.x : 0) * 16, 0);
  u32x4 av[18];
#pragma unroll
  for (int ks = 0; ks < 18; ++ks) av[ks] = bload16(dW, l * 16, (int)WS_XN + mt * 18432 + ks * 1024);
#pragma unroll
  for (int j = 0; j < 16; ++j) {
    const int idx = (w + 8 * j < 126) ? (w + 8 * j) : 125;
    blds16(dW, lds_addr_of(smem) + K1_LDS_B + idx * 1024, l * 16, (int)WS_WQ + p * 129024 + idx * 1024);
  }
  asm volatile("s_waitcnt vmcnt(34)" : "+v"(mv) :: "memory");
  if (threadIdx.x < 288) *(u32x4*)(smem + K1_LDS_PHI + threadIdx.x * 16) = mv;
  RAW_BAR();
  bf16x8 a[18];
#pragma unroll
  for (int c3 = 0; c3 < 3; ++c3) {
    if (c3 == 0) asm volatile("s_waitcnt vmcnt(28)" : "+v"(av[0]), "+v"(av[1]), "+v"(av[2]), "+v"(av[3]), "+v"(av[4]), "+v"(av[5]) :: "memory");
    if (c3 == 1) asm volatile("s_waitcnt vmcnt(22)" : "+v"(av[6]), "+v"(av[7]), "+v"(av[8]), "+v"(av[9]), "+v"(av[10]), "+v"(av[11]) :: "memory");
    if (c3 == 2) asm volatile("s_waitcnt vmcnt(16)" : "+v"(av[12]), "+v"(av[13]), "+v"(av[14]), "+v"(av[15]), "+v"(av[16]), "+v"(av[17]) :: "memory");
#pragma unroll
    for (int k6 = 0; k6 < 6; ++k6) {
      const int ks = c3 * 6 + k6;
      const char* mp = smem + K1_LDS_PHI + ks * 128 + g * 32;
      const f32x4 s0 = *(const f32x4*)(mp), s1 = *(const f32x4*)(mp + 16);
      const f32x4 c0 = *(const f32x4*)(mp + 2304), c1 = *(const f32x4*)(mp + 2304 + 16);
      u32x4 o;
      unsigned m0 = pack2(bf_lo(av[ks][0]) * c0[0], bf_hi(av[ks][0]) * c0[1]), m1 = pack2(bf_lo(av[ks][1]) * c0[2], bf_hi(av[ks][1]) * c0[3]);
      unsigned m2 = pack2(bf_lo(av[ks][2]) * c1[0], bf_hi(av[ks][2]) * c1[1]), m3 = pack2(bf_lo(av[ks][3]) * c1[2], bf_hi(av[ks][3]) * c1[3]);
      o[0] = pack2(bf_lo(m0) + s0[0], bf_hi(m0) + s0[1]);
      o[1] = pack2(bf_lo(m1) + s0[2], bf_hi(m1) + s0[3]);
      o[2] = pack2(bf_lo(m2) + s1[0], bf_hi(m2) + s1[1]);
      o[3] = pack2(bf_lo(m3) + s1[2], bf_hi(m3) + s1[3]);
      a[ks] = __builtin_bit_cast(bf16x8, o);
    }
  }
  STAMP(1, 1);
  f32x4 acc[7];
#pragma unroll
  for (int nt = 0; nt < 7; ++nt) acc[nt] = (f32x4){0, 0, 0, 0};
  bf16x8 bfr[7];
  const char* bp = smem + K1_LDS_B + l * 16;
#pragma unroll
  for (int hf = 0; hf < 2; ++hf) {
    if (hf == 0) WAIT_V(8); else WAIT_V(0);
    RAW_BAR();
    if (hf == 0) STAMP(1, 2);
#pragma unroll
    for (int nt = 0; nt < 7; ++nt) bfr[nt] = *(const bf16x8*)(bp + (hf * 63 + nt) * 1024);
    __builtin_amdgcn_sched_barrier(0);
#pragma unroll
    for (int k9 = 0; k9 < 9; ++k9) {
      const int ks = hf * 9 + k9;
#pragma unroll
      for (int nt = 0; nt < 7; ++nt) {
        acc[nt] = mfma16(bfr[nt], a[ks], acc[nt]);
        if (k9 < 8) bfr[nt] = *(const bf16x8*)(bp + ((ks + 1) * 7 + nt) * 1024);
        __builtin_amdgcn_sched_barrier(0);
      }
    }
  }
  bf16x8 wph[4][2];
#pragma unroll
  for (int s = 0; s < 4; ++s)
#pragma unroll
    for (int ft = 0; ft < 2; ++ft) wph[s][ft] = *(const bf16x8*)(ws + WS_WPHI + ((size_t)(p * 4 + s) * 2 + ft) * 1024 + l * 16);
  unsigned pk[7][2];
#pragma unroll
  for (int nt = 0; nt < 7; ++nt) {
    pk[nt][0] = pack2(acc[nt][0], acc[nt][1]);
    pk[nt][1] = pack2(acc[nt][2], acc[nt][3]);
  }
  const int tl = w * 16 + tok;
#pragma unroll
  for (int nt = 0; nt < 3; ++nt) *(u32x2*)(smem + K1_LDS_V + tl * 96 + nt * 32 + g * 8) = (u32x2){pk[nt][0], pk[nt][1]};
#pragma unroll
  for (int hh = 0; hh < 2; ++hh) {
    const int nt0 = 4 + hh;
    Feat f = feature_map(wph[2 + hh][0], wph[2 + hh][1], mk8(pk[nt0][0], pk[nt0][1], pk[nt0 + 1][0], pk[nt0 + 1][1]), g);
    char* row = smem + K1_LDS_PHI + tl * 144 + hh * 72;
    *(u32x2*)(row + g * 8) = (u32x2){f.pos01, f.pos23};
  STAMP(1, 3);
  STAMP(1, 4);
    *(u32x2*)(row + 32 + g * 8) = (u32x2){f.neg01, f.neg23};
    if (g == 0) *(u32x2*)(row + 64) = (u32x2){f.ex0, f.ex1};
  }
  __syncthreads();

  const __amdgpu_buffer_rsrc_t kv_rsrc = __builtin_amdgcn_make_buffer_rsrc(ws + WS_KVP + (size_t)p * 16 * 12288, 0, 16 * 12288, 0x00020000);
  const int hw = w & 1, tau = w >> 1;
  const bool vrow = (tau < 2) || (g == 0);
  gu32* cnt = (gu32*)(ws + WS_FLAGS + OFF_CNT1 + p * 256);
  if (w < 6) {
    const int q = (l >> 2) & 3, pp = l & 3;
    f32x4 d0 = {0, 0, 0, 0}, d1 = {0, 0, 0, 0};
#pragma unroll
    for (int k4 = 0; k4 < 4; ++k4) {
      const int tk = k4 * 32 + g * 8 + q;
      const char* pa = smem + K1_LDS_PHI + tk * 144 + hw * 72 + (16 * tau + 4 * pp) * 2;
      bf16x8 af = cat44(tr_read(pa), tr_read(pa + 4 * 144));
      const char* pv = smem + K1_LDS_V + tk * 96 + (20 * hw + 4 * pp) * 2;
      bf16x8 v0 = cat44(tr_read(pv), tr_read(pv + 4 * 96));
      bf16x8 v1 = cat44(tr_read(pv + 32), tr_read(pv + 32 + 4 * 96));
      if (tok == 2) v1 = mk8(0x3F803F80u, 0x3F803F80u, 0x3F803F80u, 0x3F803F80u);
      d0 = mfma16(af, v0, d0);
      d1 = mfma16(af, v1, d1);
    }
    const unsigned off = (unsigned)(T * 12288 + w * 2048 + l * 16);
    if (vrow) __builtin_amdgcn_raw_buffer_store_b128(__builtin_bit_cast(u32x4, d0), kv_rsrc, off, 0, 16);
    if (vrow && tok < 3) __builtin_amdgcn_raw_buffer_store_b128(__builtin_bit_cast(u32x4, d1), kv_rsrc, off + 1024, 0, 16);
    WAIT_V(0);
  STAMP(1, 5);
    if (l == 0) {
      unsigned old = __hip_atomic_fetch_add((LAS unsigned*)(smem + K1_LDS_MISC), 1u, __ATOMIC_RELAXED, __HIP_MEMORY_SCOPE_WORKGROUP);
      if (old == 5) __hip_atomic_fetch_add(cnt, 1u, RLX_AGENT);
    }
  }
  bf16x8 qb1[2], qb2[2];
#pragma unroll
  STAMP(1, 6);
  for (int hh = 0; hh < 2; ++hh) {
    const int nt0 = 2 + hh;
  STAMP(1, 7);
    Feat f = feature_map(wph[hh][0], wph[hh][1], mk8(pk[nt0][0], pk[nt0][1], pk[nt0 + 1][0], pk[nt0 + 1][1]), g);
    qb1[hh] = mk8(f.pos01, f.pos23, f.neg01, f.neg23);
    qb2[hh] = mk8(f.ex0, f.ex1, 0u, 0u);
  }
  if (w == 7) wait_ge(cnt, 16u);
  __builtin_amdgcn_fence(__ATOMIC_ACQUIRE, "wavefront");
  __syncthreads();
  if (w < 6) {
    f32x4 s0 = {0, 0, 0, 0}, s1 = {0, 0, 0, 0};
    const unsigned off = (unsigned)(w * 2048 + l * 16);
    {
      u32x4 r0[16], r1[16];
      const unsigned o0 = vrow ? off : 0x7ffffff0u, o1 = (vrow && tok < 3) ? off + 1024 : 0x7ffffff0u;
#pragma unroll
      for (int t = 0; t < 16; ++t) {
        r0[t] = __builtin_amdgcn_raw_buffer_load_b128(kv_rsrc, o0, t * 12288, 16);
        r1[t] = __builtin_amdgcn_raw_buffer_load_b128(kv_rsrc, o1, t * 12288, 16);
      }
#pragma unroll
      for (int t = 0; t < 16; ++t) { s0 += __builtin_bit_cast(f32x4, r0[t]); s1 += __builtin_bit_cast(f32x4, r1[t]); }
    }
    u32x2 k0 = {pack2(s0[0], s0[1]), pack2(s0[2], s0[3])};
    u32x2 k1 = {pack2(s1[0], s1[1]), pack2(s1[2], s1[3])};
    if (tau < 2) {
  STAMP(1, 8);
      *(u32x2*)(smem + (hw * 2 + 0) * 1024 + l * 16 + tau * 8) = k0;
      *(u32x2*)(smem + (hw * 2 + 1) * 1024 + l * 16 + tau * 8) = k1;
    } else {
      *(u32x4*)(smem + 4096 + (hw * 2 + 0) * 1024 + l * 16) = (u32x4){k0[0], k0[1], 0u, 0u};
      *(u32x4*)(smem + 4096 + (hw * 2 + 1) * 1024 + l * 16) = (u32x4){k1[0], k1[1], 0u, 0u};
    }
  }
  __syncthreads();
  char* obase = ws + WS_O + (size_t)mt * 18432;
#pragma unroll
  for (int hh = 0; hh < 2; ++hh) {
    f32x4 o0 = {0, 0, 0, 0}, o1 = {0, 0, 0, 0};
    o0 = mfma16(*(const bf16x8*)(smem + (hh * 2 + 0) * 1024 + l * 16), qb1[hh], o0);
    o1 = mfma16(*(const bf16x8*)(smem + (hh * 2 + 1) * 1024 + l * 16), qb1[hh], o1);
    o0 = mfma16(*(const bf16x8*)(smem + 4096 + (hh * 2 + 0) * 1024 + l * 16), qb2[hh], o0);
    o1 = mfma16(*(const bf16x8*)(smem + 4096 + (hh * 2 + 1) * 1024 + l * 16), qb2[hh], o1);
    float den = __shfl(o1[2], tok);
    den = rbf(rbf(den) + 1e-6f);
    const float rd = __builtin_amdgcn_rcpf(den);
    const int c0 = 36 * p + 18 * hh + 4 * g;
    unsigned u0 = pack2(rbf(o0[0]) * rd, rbf(o0[1]) * rd), u1 = pack2(rbf(o0[2]) * rd, rbf(o0[3]) * rd);
    {
  STAMP(1, 9); STAMPR(1, 15);
      int cA = c0, cB = c0 + 2;
      *(unsigned*)(obase + (cA >> 5) * 1024 + (tok + 16 * ((cA >> 3) & 3)) * 16 + (cA & 7) * 2) = u0;
      *(unsigned*)(obase + (cB >> 5) * 1024 + (tok + 16 * ((cB >> 3) & 3)) * 16 + (cB & 7) * 2) = u1;
    }
    if (g == 0) {
      int cC = 36 * p + 18 * hh + 16;
      *(unsigned*)(obase + (cC >> 5) * 1024 + (tok + 16 * ((cC >> 3) & 3)) * 16 + (cC & 7) * 2) = pack2(rbf(o1[0]) * rd, rbf(o1[1]) * rd);
    }
  }
}

  STAMP_DECL; STAMPR(2, 14); STAMP(2, 0);
constexpr int K2_LDS = 36864 + 9 * 32 * 8 + 32 * 8;

__global__ __launch_bounds__(576) void k2_oproj(const unsigned short* __restrict__ x, char* __restrict__ ws) {
  extern __shared__ __attribute__((aligned(16))) char smem[];
  const int b = blockIdx.x, xcd = b & 7, bi = b >> 3;
  const int tt = xcd * 8 + (bi >> 2), cg = bi & 3;
  const int w = __builtin_amdgcn_readfirstlane(threadIdx.x >> 6), l = threadIdx.x & 63;
  const int tok = l & 15, g = l >> 4;
  {
    const char* src = ws + WS_O + (size_t)tt * 36864 + l * 16;
#pragma unroll
    for (int j = 0; j < 4; ++j) glds16(src + (w + 9 * j) * 1024, smem + (w + 9 * j) * 1024);
  }
  const int nt = cg * 9 + w;
  bf16x8 bw[18];
  {
    const char* src = ws + WS_WO + (size_t)nt * 18432 + l * 16;
#pragma unroll
    for (int ks = 0; ks < 18; ++ks) bw[ks] = *(const bf16x8*)(src + ks * 1024);
  STAMP(2, 1);
  }
  const int n0 = nt * 16 + g * 4;
  u32x2 xr[2];
#pragma unroll
  for (int m = 0; m < 2; ++m) xr[m] = *(const u32x2*)(x + (size_t)(tt * 32 + m * 16 + tok) * 576 + n0);
  const float* modf = (const float*)(ws + WS_MODF);
  const f32x4 ga = *(const f32x4*)(modf + 1152 + n0);
  const f32x4 sb = *(const f32x4*)(modf + 1728 + n0);
  const f32x4 c1 = *(const f32x4*)(modf + 2304 + n0);
  WAIT_V(0);
  __syncthreads();
  f32x4 acc[2] = {{0, 0, 0, 0}, {0, 0, 0, 0}};
  {
    bf16x8 ar[4][2];
#pragma unroll
    for (int k = 0; k < 4; ++k)
#pragma unroll
      for (int m = 0; m < 2; ++m) ar[k][m] = *(const bf16x8*)(smem + (m * 18 + k) * 1024 + l * 16);
    __builtin_amdgcn_sched_barrier(0);
#pragma unroll
    for (int ks = 0; ks < 18; ++ks) {
#pragma unroll
      for (int m = 0; m < 2; ++m) {
        acc[m] = mfma16(bw[ks], ar[ks & 3][m], acc[m]);
        if (ks + 4 < 18) ar[ks & 3][m] = *(const bf16x8*)(smem + (m * 18 + ks + 4) * 1024 + l * 16);
        __builtin_amdgcn_sched_barrier(0);
      }
    }
  }
  float x1[2][4];
  float* stat = (float*)(smem + 36864);
  float* fin = (float*)(smem + 36864 + 2304);
#pragma unroll
  for (int m = 0; m < 2; ++m) {
    float xin[4] = {bf_lo(xr[m][0]), bf_hi(xr[m][0]), bf_lo(xr[m][1]), bf_hi(xr[m][1])};
  STAMP(2, 2);
  STAMP(2, 3);
#pragma unroll
    for (int i = 0; i < 4; ++i) x1[m][i] = rbf(xin[i] + rbf(ga[i] * rbf(acc[m][i])));
    float s = gsum((x1[m][0] + x1[m][1]) + (x1[m][2] + x1[m][3]));
    float mu = s * (1.0f / 16.0f);
    float q = 0;
#pragma unroll
    for (int i = 0; i < 4; ++i) q += (x1[m][i] - mu) * (x1[m][i] - mu);
    q = gsum(q);
    if (g == 0) { stat[(w * 32 + m * 16 + tok) * 2] = mu; stat[(w * 32 + m * 16 + tok) * 2 + 1] = q; }
    *(u32x2*)(ws + WS_X1 + ((size_t)(tt * 32 + m * 16 + tok) * 576 + n0) * 2) = (u32x2){pack2(x1[m][0], x1[m][1]), pack2(x1[m][2], x1[m][3])};
  }
  __syncthreads();
  if (w == 0) {
    const int tk = l & 31;
    float mu = 0;
#pragma unroll
    for (int i = 0; i < 9; ++i) mu += stat[(i * 32 + tk) * 2];
    mu *= (1.0f / 9.0f);
    float m2 = 0;
#pragma unroll
    for (int i = 0; i < 9; ++i) {
      float dm = stat[(i * 32 + tk) * 2] - mu;
      m2 += stat[(i * 32 + tk) * 2 + 1] + 16.0f * dm * dm;
    }
    gu64* gr = (gu64*)(ws + WS_FLAGS + OFF_GRAN2) + (size_t)tt * 256;
    const float mine = (l < 32) ? mu : m2;
    __hip_atomic_store(gr + cg * 64 + l, (1ull << 32) | (unsigned long long)__float_as_uint(mine), RLX_AGENT);
    float val[4];
    for (unsigned spins = 0; spins < kSpinCap; ++spins) {
      bool ok = true;
#pragma unroll
      for (int j = 0; j < 4; ++j) {
        unsigned long long v = __hip_atomic_load(gr + j * 64 + l, RLX_AGENT);
        val[j] = __uint_as_float((unsigned)v);
        ok &= ((v >> 32) == 1ull);
      }
      if (__all(ok)) break;
      __builtin_amdgcn_s_sleep(1);
    }
  STAMP(2, 4);
    float m2v[4];
#pragma unroll
    for (int j = 0; j < 4; ++j) m2v[j] = __shfl(val[j], tk + 32);
    float mean = ((val[0] + val[1]) + (val[2] + val[3])) * 0.25f;
    float M2 = 0;
#pragma unroll
    for (int j = 0; j < 4; ++j) { float dm = val[j] - mean; M2 += m2v[j] + 144.0f * dm * dm; }
    const float var = M2 * (1.0f / 576.0f);
    if (l < 32) {
  STAMP(2, 5); STAMPR(2, 15);
      fin[tk * 2] = rbf(mean);
      fin[tk * 2 + 1] = 1.0f / rbf(sqrtf(rbf(rbf(var) + 1e-5f)));
    }
  }
  __syncthreads();
#pragma unroll
  for (int m = 0; m < 2; ++m) {
    const float mb = fin[(m * 16 + tok) * 2], rs = fin[(m * 16 + tok) * 2 + 1];
    float h[4];
#pragma unroll
    for (int i = 0; i < 4; ++i) h[i] = rbf(rbf(rbf(x1[m][i] - mb) * rs) * c1[i]) + sb[i];
    char* dst = ws + WS_H2 + ((size_t)(tt * 2 + m) * 18 + (nt >> 1)) * 1024 + (tok + 16 * (2 * (nt & 1) + (g >> 1))) * 16 + (g & 1) * 8;
    *(u32x2*)dst = (u32x2){pack2(h[0], h[1]), pack2(h[2], h[3])};
  }
}

constexpr int K3_STAGE = 34816, K3_LDS = 4 * K3_STAGE;

__device__ __forceinline__ float geglu(float x, float u) {
  const float k1 = -2.0f * 0.7978845608028654f * 1.4426950408889634f, k2 = k1 * 0.044715f;
  const float e = __builtin_amdgcn_exp2f(x * __builtin_fmaf(x * x, k2, k1));
  return x * __builtin_amdgcn_rcpf(1.0f + e) * u;
}

template <int NT>
__device__ __forceinline__ void k3_body(char* __restrict__ ws, char* smem, u32x4 dA, u32x4 dG, u32x4 dU, const int (&voff)[5], int w, int l,
                                        int T8, int cgp) {
  const int wm = w & 3, wn = w >> 2;
  const int g = l >> 4, q = (l >> 2) & 3, pp = l & 3;
  const bool five = (w < 2);
  const unsigned lb = lds_addr_of(smem) + w * 1024;
  const u32x4 d3 = (w == 0) ? dG : dU;
  auto piece = [&](int ks, int i) {
    const unsigned base = lb + (ks & 3) * K3_STAGE;
    if (i == 0) blds16(dA, base, voff[0], ks * 1024);
    if (i == 1) blds16(dA, base + 8192, voff[1], ks * 1024);
    if (i == 2) blds16(dG, base + 16384, voff[2], ks * 294912);
    if (i == 3) blds16(d3, base + 24576, voff[3], ks * 294912);
    if (i == 4) { if (five) blds16(dU, base + 32768, voff[4], ks * 294912); }
  };
  STAMP_DECL; STAMPR(3, 14); STAMP(3, 0);
#pragma unroll
  for (int ks = 0; ks < 4; ++ks)
#pragma unroll
    for (int i = 0; i < 5; ++i) piece(ks, i);

  f32x4 accg[4][NT], accu[4][NT];
#pragma unroll
  for (int m = 0; m < 4; ++m)
#pragma unroll
    for (int n = 0; n < NT; ++n) { accg[m][n] = (f32x4){0, 0, 0, 0}; accu[m][n] = (f32x4){0, 0, 0, 0}; }

  const int aoff = wm * 4096 + l * 16;
  const int boff0 = 16384 + 288 * (8 * g + q + 4 * (g & 1)) + 8 * pp + wn * 160;
  const int boff1 = 16384 + 288 * (8 * g + q + 4 * ((g & 1) ^ 1)) + 8 * pp + wn * 160;

  bf16x8 af[4], bg[NT], bu[NT];
  if (five) WAIT_V(15); else WAIT_V(12);
  RAW_BAR();
  STAMP(3, 1);
#pragma unroll
  for (int m = 0; m < 4; ++m) af[m] = *(const bf16x8*)(smem + aoff + m * 1024);
#pragma unroll
  for (int n = 0; n < NT; ++n) {
    bg[n] = cat44(tr_read(smem + boff0 + n * 32), tr_read(smem + boff1 + n * 32));
    bu[n] = cat44(tr_read(smem + boff0 + 9216 + n * 32), tr_read(smem + boff1 + 9216 + n * 32));
  }
  auto step = [&](int ks, bool dma, bool more) {
    const int so = ((ks + 1) & 3) * K3_STAGE;
    const int oa = aoff + so, o0 = boff0 + so, o1 = boff1 + so;
    __builtin_amdgcn_sched_barrier(0);
#pragma unroll
    for (int n = 0; n < NT; ++n) {
#pragma unroll
      for (int m = 0; m < 4; ++m) {
        accg[m][n] = mfmaL(bg[n], af[m], accg[m][n]);
        accu[m][n] = mfmaL(bu[n], af[m], accu[m][n]);
        if (n == NT - 1 && more) {
          __builtin_amdgcn_sched_barrier(0);
          af[m] = *(const bf16x8*)(smem + oa + m * 1024);
          __builtin_amdgcn_sched_barrier(0);
        }
      }
      __builtin_amdgcn_sched_barrier(0);
      if (more) {
        bg[n] = cat44(tr_read(smem + o0 + n * 32), tr_read(smem + o1 + n * 32));
        bu[n] = cat44(tr_read(smem + o0 + 9216 + n * 32), tr_read(smem + o1 + 9216 + n * 32));
      }
#if !(ABL & 1)
      if (dma) { piece(ks + 4, n); if (NT == 4 && n == 3) piece(ks + 4, 4); }
#endif
      __builtin_amdgcn_sched_barrier(0);
    }
  };
#pragma unroll 1
  for (int ks = 0; ks < 17; ++ks) {
    if (ks < 15) { if (five) WAIT_V(10); else WAIT_V(8); }
    else if (ks == 15) { if (five) WAIT_V(5); else WAIT_V(4); }
    else WAIT_V(0);
    RAW_BAR();
    step(ks, ks < 14, !(ABL & 8));
  }
  step(17, false, false);
  STAMP(3, 3);
  int lz = threadIdx.x;
  asm volatile("" : "+v"(lz));
  const int lofs = (lz & 15) * 16 + ((lz >> 5) & 1) * 256 + ((lz >> 4) & 1) * 8;
#pragma unroll
  for (int m = 0; m < 4; ++m) {
#pragma unroll
    for (int n = 0; n < NT; ++n) {
      float h[4];
#pragma unroll
      for (int i = 0; i < 4; ++i) h[i] = geglu(accg[m][n][i], accu[m][n][i]);
      const int ntg = cgp * 9 + wn * 5 + n;
      char* dst = ws + WS_HH + ((size_t)(T8 * 16 + wm * 4 + m) * 144 + (ntg >> 1)) * 1024 + (ntg & 1) * 512 + lofs;
      *(u32x2*)dst = (u32x2){pack2(h[0], h[1]), pack2(h[2], h[3])};
      if (n & 1) __builtin_amdgcn_sched_barrier(0);
    }
  }
  STAMP(3, 4); STAMPR(3, 15);
}

__global__ __launch_bounds__(512, 2) __attribute__((amdgpu_waves_per_eu(2, 2))) void k3_gateup(const unsigned short* __restrict__ wg, const unsigned short* __restrict__ wu,
                                                    char* __restrict__ ws) {
  extern __shared__ __attribute__((aligned(16))) char smem[];
  const int b = blockIdx.x, xcd = b & 7, bi = b >> 3;
  const int T8 = (xcd >> 2) * 4 + (bi & 3), cgp = (xcd & 3) * 8 + (bi >> 2);
  const int w = __builtin_amdgcn_readfirstlane(threadIdx.x >> 6), l = threadIdx.x & 63;
  int voff[5];
#pragma unroll
  for (int i = 0; i < 5; ++i) {
    const int s = i * 8 + w;
    if (i < 2) {
      voff[i] = (T8 * 16 + s) * 18432 + l * 16;
    } else {
      const int sj = (s < 34) ? s - 16 : 17;
      const int j = (sj < 9) ? sj : sj - 9;
      const int idx = j * 64 + l, pos = idx / 18, c = idx % 18;
      const int kr = pos ^ (((pos >> 3) & 1) << 2);
      voff[i] = kr * 9216 + cgp * 288 + c * 16;
    }
  }
  const u32x4 dA = mk_desc(ws + WS_H2, (unsigned)FRAG_ACT), dG = mk_desc(wg, 576u * 9216u), dU = mk_desc(wu, 576u * 9216u);
  if (w < 4) k3_body<5>(ws, smem, dA, dG, dU, voff, w, l, T8, cgp);
  else k3_body<4>(ws, smem, dA, dG, dU, voff, w, l, T8, cgp);
}

constexpr int K4_STAGE = 34816, K4_LDS = 4 * K4_STAGE + 64;

__global__ __launch_bounds__(512, 2) __attribute__((amdgpu_waves_per_eu(2, 2))) void k4_down(const unsigned short* __restrict__ wd, char* __restrict__ ws,
                                                  unsigned short* __restrict__ out) {
  extern __shared__ __attribute__((aligned(16))) char smem[];
  const int b = blockIdx.x, xcd = b & 7, bi = b >> 3;
  const int js = xcd & 3, T = (xcd >> 2) * 8 + (bi & 7), cg = bi >> 3;
  const int w = __builtin_amdgcn_readfirstlane(threadIdx.x >> 6), l = threadIdx.x & 63;
  const int wm = w & 3, kk = w >> 2;
  const int tok = l & 15, g = l >> 4, q = (l >> 2) & 3, pp = l & 3;
  if (threadIdx.x == 0) *(LAS unsigned*)(smem + 4 * K4_STAGE) = 0;

  const u32x4 rA = mk_desc(ws + WS_HH, 128u * 144u * 1024u);
  const u32x4 rB = mk_desc(wd, 4608u * 1152u);
  int voff[5];
#pragma unroll
  STAMP_DECL; STAMPR(4, 14); STAMP(4, 0);
  for (int i = 0; i < 5; ++i) {
    const int s = i * 8 + w;
    if (i < 2) {
      voff[i] = ((T * 8 + (s >> 1)) * 144 + js * 36 + (s & 1)) * 1024 + l * 16;
    } else {
      const int j = (s < 34) ? s - 16 : 17;
      const int idx = j * 64 + l, pos = idx / 18, c = idx % 18;
      const int kr = pos ^ (((pos >> 3) & 1) << 2);
      voff[i] = (js * 1152 + kr) * 1152 + cg * 288 + c * 16;
    }
  }
  const bool five = (w < 2);
  auto piece = [&](int st, int i) {
    const unsigned base = lds_addr_of(smem) + (st & 3) * K4_STAGE + w * 1024;
    if (i == 0) blds16(rA, base, voff[0], st * 2048);
    if (i == 1) blds16(rA, base + 8192, voff[1], st * 2048);
    if (i == 2) blds16(rB, base + 16384, voff[2], st * 73728);
    if (i == 3) blds16(rB, base + 24576, voff[3], st * 73728);
    if (i == 4) { if (five) blds16(rB, base + 32768, voff[4], st * 73728); }
  };
  STAMP_DECL; STAMPR(4, 14); STAMP(4, 0);
#pragma unroll
  for (int st = 0; st < 4; ++st)
#pragma unroll
    for (int i = 0; i < 5; ++i) piece(st, i);

  f32x4 acc[2][9];
#pragma unroll
  for (int m = 0; m < 2; ++m)
#pragma unroll
    for (int n = 0; n < 9; ++n) acc[m][n] = (f32x4){0, 0, 0, 0};

  const int aoff = (wm * 4 + kk) * 1024 + l * 16;
  const int boff0 = 16384 + 288 * (32 * kk + 8 * g + q + 4 * (g & 1)) + 8 * pp;
  const int boff1 = 16384 + 288 * (32 * kk + 8 * g + q + 4 * ((g & 1) ^ 1)) + 8 * pp;

  bf16x8 af[2], bw[9];
  if (five) WAIT_V(15); else WAIT_V(12);
  RAW_BAR();
  STAMP(4, 1);
#pragma unroll
  for (int m = 0; m < 2; ++m) af[m] = *(const bf16x8*)(smem + aoff + m * 2048);
#pragma unroll
  for (int n = 0; n < 9; ++n) bw[n] = cat44(tr_read(smem + boff0 + n * 32), tr_read(smem + boff1 + n * 32));
  auto step = [&](int st, bool dma, bool more) {
    const int so = ((st + 1) & 3) * K4_STAGE;
    const int oa = aoff + so, o0 = boff0 + so, o1 = boff1 + so;
    __builtin_amdgcn_sched_barrier(0);
#pragma unroll
    for (int n3 = 0; n3 < 3; ++n3) {
      if (n3 < 2) {
#pragma unroll
        for (int nn = 0; nn < 3; ++nn)
#pragma unroll
          for (int m = 0; m < 2; ++m) acc[m][n3 * 3 + nn] = mfmaL(bw[n3 * 3 + nn], af[m], acc[m][n3 * 3 + nn]);
      } else {
#pragma unroll
        for (int m = 0; m < 2; ++m) {
#pragma unroll
          for (int nn = 0; nn < 3; ++nn) acc[m][n3 * 3 + nn] = mfmaL(bw[n3 * 3 + nn], af[m], acc[m][n3 * 3 + nn]);
          if (more) {
            __builtin_amdgcn_sched_barrier(0);
            af[m] = *(const bf16x8*)(smem + oa + m * 2048);
            __builtin_amdgcn_sched_barrier(0);
          }
        }
      }
      __builtin_amdgcn_sched_barrier(0);
      if (more) {
#pragma unroll
        for (int nn = 0; nn < 3; ++nn) bw[n3 * 3 + nn] = cat44(tr_read(smem + o0 + (n3 * 3 + nn) * 32), tr_read(smem + o1 + (n3 * 3 + nn) * 32));
      }
#if !(ABL & 1)
      if (dma) { piece(st + 4, 2 * n3); if (n3 < 2) piece(st + 4, 2 * n3 + 1); }
#endif
      __builtin_amdgcn_sched_barrier(0);
    }
  };
#pragma unroll 1
  for (int st = 0; st < 17; ++st) {
    if (st < 15) { if (five) WAIT_V(10); else WAIT_V(8); }
    else if (st == 15) { if (five) WAIT_V(5); else WAIT_V(4); }
    else WAIT_V(0);
    RAW_BAR();
    step(st, st < 14, !(ABL & 8));
  }
  step(17, false, false);
  __syncthreads();
#pragma unroll
  for (int n = 0; n < 9; ++n) *(f32x4*)(smem + (w * 9 + n) * 1024 + l * 16) = kk ? acc[0][n] : acc[1][n];
  __syncthreads();
  f32x4 r[9];
#pragma unroll
  for (int n = 0; n < 9; ++n) {
    f32x4 o = *(const f32x4*)(smem + ((w ^ 4) * 9 + n) * 1024 + l * 16);
    r[n] = kk ? (o + acc[1][n]) : (acc[0][n] + o);
  }
  const int tile = T * 4 + cg;
  const __amdgpu_buffer_rsrc_t sl = __builtin_amdgcn_make_buffer_rsrc(ws + WS_SLAB + (size_t)tile * 294912, 0, 294912, 0x00020000);
  const unsigned soff = (unsigned)(w * 9216 + l * 16);
#pragma unroll
  for (int n = 0; n < 9; ++n) {
    const int own = (n < 3) ? 0 : ((n - 1) >> 1);
  STAMP(4, 6);
    if (own != js) __builtin_amdgcn_raw_buffer_store_b128(__builtin_bit_cast(u32x4, r[n]), sl, soff + js * 73728 + n * 1024, 0, 16);
  }
  gu32* cnt = (gu32*)(ws + WS_FLAGS + OFF_CNT4 + tile * 256);
  WAIT_V(0);
  if (l == 0) {
    unsigned old = __hip_atomic_fetch_add((LAS unsigned*)(smem + 4 * K4_STAGE), 1u, __ATOMIC_RELAXED, __HIP_MEMORY_SCOPE_WORKGROUP);
    if (old == 7) __hip_atomic_fetch_add(cnt, 1u, RLX_AGENT);
  }
  const int row = T * 128 + (wm * 2 + kk) * 16 + tok;
  const int nfirst = (js == 0) ? 0 : (2 * js + 1), ncnt = (js == 0) ? 3 : 2;
  const float* gbp = (const float*)(ws + WS_MODF) + 2880;
  u32x2 xr[3];
  f32x4 gb[3];
#pragma unroll
  for (int j = 0; j < 3; ++j) {
    if (j < ncnt) {
      const int n0 = cg * 144 + (nfirst + j) * 16 + g * 4;
  STAMP(4, 7); STAMPR(4, 15);
      xr[j] = *(const u32x2*)(ws + WS_X1 + ((size_t)row * 576 + n0) * 2);
      gb[j] = *(const f32x4*)(gbp + n0);
    }
  }
  if (w == 0) wait_ge(cnt, 4u);
  __builtin_amdgcn_fence(__ATOMIC_ACQUIRE, "wavefront");
  __syncthreads();
#pragma unroll
  for (int n = 0; n < 9; ++n) {
    const int own = (n < 3) ? 0 : ((n - 1) >> 1);
    if (own == js) {
      const int j = n - ((own == 0) ? 0 : (2 * own + 1));
      f32x4 pv[4];
#pragma unroll
      for (int s = 0; s < 4; ++s) {
        if (s != own) pv[s] = __builtin_bit_cast(f32x4, __builtin_amdgcn_raw_buffer_load_b128(sl, soff + s * 73728 + n * 1024, 0, 16));
        else pv[s] = r[n];
      }
      f32x4 y = (pv[0] + pv[1]) + (pv[2] + pv[3]);
      float xin[4] = {bf_lo(xr[j][0]), bf_hi(xr[j][0]), bf_lo(xr[j][1]), bf_hi(xr[j][1])};
      float o[4];
#pragma unroll
      for (int i = 0; i < 4; ++i) o[i] = xin[i] + rbf(gb[j][i] * rbf(y[i]));
      *(u32x2*)(out + (size_t)row * 576 + cg * 144 + n * 16 + g * 4) = (u32x2){pack2(o[0], o[1]), pack2(o[2], o[3])};
    }
  }
}

extern "C" void kernel_launch(void* const* d_in, const int* in_sizes, int n_in,
                              void* d_out, int out_size, void* d_ws, size_t ws_size,
                              hipStream_t stream) {
  const unsigned short* x = (const unsigned short*)d_in[0];
  const unsigned short* wqkv = (const unsigned short*)d_in[1];
  const unsigned short* wphi = (const unsigned short*)d_in[2];
  const unsigned short* wo = (const unsigned short*)d_in[3];
  const unsigned short* wg = (const unsigned short*)d_in[5];
  const unsigned short* wu = (const unsigned short*)d_in[6];
  const unsigned short* wd = (const unsigned short*)d_in[7];
  const unsigned short* c = (const unsigned short*)d_in[8];
  const unsigned short* wmod = (const unsigned short*)d_in[9];
  char* ws = (char*)d_ws;
  (void)hipFuncSetAttribute((const void*)k1_attn, hipFuncAttributeMaxDynamicSharedMemorySize, K1_LDS);
  (void)hipFuncSetAttribute((const void*)k3_gateup, hipFuncAttributeMaxDynamicSharedMemorySize, K3_LDS);
  (void)hipFuncSetAttribute((const void*)k4_down, hipFuncAttributeMaxDynamicSharedMemorySize, K4_LDS);
  k0_prep<<<K0_GRID, 256, 0, stream>>>(x, wqkv, wphi, wo, c, wmod, ws);
  k1_attn<<<256, 512, K1_LDS, stream>>>(ws);
  k2_oproj<<<256, 576, K2_LDS, stream>>>(x, ws);
  k3_gateup<<<256, 512, K3_LDS, stream>>>(wg, wu, ws);
  k4_down<<<256, 512, K4_LDS, stream>>>(wd, ws, (unsigned short*)d_out);
}
```

```cpp
#include <hip/hip_runtime.h>
#include <hip/hip_bf16.h>
#include <stdint.h>

typedef short bf16x8 __attribute__((ext_vector_type(8)));
typedef short bf16x4 __attribute__((ext_vector_type(4)));
typedef float f32x4 __attribute__((ext_vector_type(4)));
typedef float f32x2 __attribute__((ext_vector_type(2)));
typedef unsigned u32x4 __attribute__((ext_vector_type(4)));
typedef unsigned u32x2 __attribute__((ext_vector_type(2)));
typedef __bf16 bfv2 __attribute__((ext_vector_type(2)));
#define GAS __attribute__((address_space(1)))
#define LAS __attribute__((address_space(3)))
typedef GAS unsigned gu32;
typedef GAS unsigned long long gu64;
#define RLX_AGENT __ATOMIC_RELAXED, __HIP_MEMORY_SCOPE_AGENT

constexpr int S_ = 2048, D_ = 576, DF_ = 4608;

constexpr size_t WS_FLAGS = 0;
constexpr size_t WS_FLAGS_BYTES = 196608;
constexpr size_t OFF_CNT1 = 0;
constexpr size_t OFF_CNTT = 24576;
constexpr size_t OFF_CNT4 = 4096;
constexpr size_t OFF_GRAN2 = 65536;
constexpr size_t WS_MODF = WS_FLAGS + WS_FLAGS_BYTES;
constexpr size_t WS_MODH = WS_MODF + 16384;
constexpr size_t WS_XN = WS_MODH + 8192;
constexpr size_t FRAG_ACT = 128 * 18 * 1024;
constexpr size_t WS_WQ = WS_XN + FRAG_ACT;
constexpr size_t WS_WO = WS_WQ + 16 * 126 * 1024;
constexpr size_t WS_WPHI = WS_WO + 36 * 18 * 1024;
constexpr size_t WS_KVP = WS_WPHI + 16 * 8 * 1024;
constexpr size_t WS_O = WS_KVP + 16 * 16 * 12 * 1024;
constexpr size_t WS_X1 = WS_O + FRAG_ACT;
constexpr size_t WS_H2 = WS_X1 + FRAG_ACT;
constexpr size_t WS_HH = WS_H2 + FRAG_ACT;
constexpr size_t WS_SLAB = WS_HH + (size_t)128 * 144 * 1024;
constexpr size_t WS_WD = WS_SLAB + (size_t)64 * 4 * 8 * 9 * 1024;
constexpr size_t WS_END = WS_WD + (size_t)5184 * 1024;

__device__ __forceinline__ float bf_lo(unsigned u) { return __uint_as_float(u << 16); }
__device__ __forceinline__ float bf_hi(unsigned u) { return __uint_as_float(u & 0xffff0000u); }
__device__ __forceinline__ float bf2f(unsigned short u) { return __uint_as_float(((unsigned)u) << 16); }
__device__ __forceinline__ unsigned pack2(float a, float b) {
  f32x2 v = {a, b};
  bfv2 r = __builtin_convertvector(v, bfv2);
  return __builtin_bit_cast(unsigned, r);
}
__device__ __forceinline__ float rbf(float a) { return (float)(__bf16)a; }
__device__ __forceinline__ f32x4 mfma16(bf16x8 a, bf16x8 b, f32x4 c) {
  return __builtin_amdgcn_mfma_f32_16x16x32_bf16(a, b, c, 0, 0, 0);
}
__device__ __forceinline__ bf16x8 mk8(unsigned a, unsigned b, unsigned c, unsigned d) {
  u32x4 v = {a, b, c, d};
  return __builtin_bit_cast(bf16x8, v);
}
__device__ __forceinline__ bf16x4 tr_read(const char* p) {
  return __builtin_amdgcn_ds_read_tr16_b64_v4i16((LAS bf16x4*)p);
}
__device__ __forceinline__ bf16x8 cat44(bf16x4 a, bf16x4 b) {
  return __builtin_shufflevector(a, b, 0, 1, 2, 3, 4, 5, 6, 7);
}
__device__ __forceinline__ void glds16(const void* g, void* l) {
  __builtin_amdgcn_global_load_lds((const GAS unsigned*)g, (LAS unsigned*)l, 16, 0, 0);
}
__device__ __forceinline__ __amdgpu_buffer_rsrc_t mk_rsrc(const void* base, unsigned bytes) {
  return __builtin_amdgcn_make_buffer_rsrc((void*)base, 0, bytes, 0x00020000);
}
__device__ __forceinline__ u32x4 mk_desc(const void* base, unsigned bytes) {
  const unsigned long long a = (unsigned long long)base;
  return (u32x4){(unsigned)a, (unsigned)(a >> 32) & 0xffffu, bytes, 0x00020000u};
}
__device__ __forceinline__ void blds16(u32x4 desc, unsigned lds_addr, int voff, int soff) {
  unsigned keep;
  asm volatile("s_mov_b32 %0, m0\n\ts_mov_b32 m0, %2\n\ts_nop 0\n\tbuffer_load_dwordx4 %1, %3, %4 offen lds\n\ts_mov_b32 m0, %0"
               : "=&s"(keep) : "v"(voff), "s"(lds_addr), "s"(desc), "s"(soff) : "memory");
}
__device__ __forceinline__ u32x4 bload16(u32x4 desc, int voff, int soff) {
  u32x4 r;
  asm volatile("buffer_load_dwordx4 %0, %1, %2, %3 offen" : "=v"(r) : "v"(voff), "s"(desc), "s"(soff) : "memory");
  return r;
}
__device__ __forceinline__ unsigned lds_addr_of(const void* p) { return (unsigned)(unsigned long long)(LAS const char*)p; }
template <bool WT> __device__ __forceinline__ void st8(void* p, u32x2 v) {
  if (WT) __hip_atomic_store((gu64*)p, (unsigned long long)v[0] | ((unsigned long long)v[1] << 32), RLX_AGENT);
  else *(u32x2*)p = v;
}
template <bool WT> __device__ __forceinline__ void st16(char* base, size_t off, u32x4 v) {
  if (WT) __builtin_amdgcn_raw_buffer_store_b128(v, __builtin_amdgcn_make_buffer_rsrc(base, 0, 0x7fffffff, 0x00020000), (unsigned)off, 0, 16);
  else *(u32x4*)(base + off) = v;
}
__device__ __forceinline__ float gmax(float v) {
  auto r = __builtin_amdgcn_permlane16_swap(__float_as_uint(v), __float_as_uint(v), false, false);
  v = fmaxf(__uint_as_float(r[0]), __uint_as_float(r[1]));
  auto q = __builtin_amdgcn_permlane32_swap(__float_as_uint(v), __float_as_uint(v), false, false);
  return fmaxf(__uint_as_float(q[0]), __uint_as_float(q[1]));
}
__device__ __forceinline__ float gmin(float v) {
  auto r = __builtin_amdgcn_permlane16_swap(__float_as_uint(v), __float_as_uint(v), false, false);
  v = fminf(__uint_as_float(r[0]), __uint_as_float(r[1]));
  auto q = __builtin_amdgcn_permlane32_swap(__float_as_uint(v), __float_as_uint(v), false, false);
  return fminf(__uint_as_float(q[0]), __uint_as_float(q[1]));
}
__device__ __forceinline__ float gsum(float v) {
  auto r = __builtin_amdgcn_permlane16_swap(__float_as_uint(v), __float_as_uint(v), false, false);
  v = __uint_as_float(r[0]) + __uint_as_float(r[1]);
  auto q = __builtin_amdgcn_permlane32_swap(__float_as_uint(v), __float_as_uint(v), false, false);
  return __uint_as_float(q[0]) + __uint_as_float(q[1]);
}
#define WAIT_V(n) asm volatile("s_waitcnt vmcnt(" #n ")" ::: "memory")
#define WAIT_L0() asm volatile("s_waitcnt lgkmcnt(0)" ::: "memory")
#define RAW_BAR() do { asm volatile("s_waitcnt lgkmcnt(0)" ::: "memory"); __builtin_amdgcn_s_barrier(); } while (0)
constexpr unsigned kSpinCap = 1u << 20;

__device__ __forceinline__ void wait_ge(gu32* word, unsigned want) {
  for (unsigned spins = 0; spins < kSpinCap; ++spins) {
    unsigned v = (unsigned)__builtin_amdgcn_readfirstlane(__hip_atomic_load(word, RLX_AGENT));
    if (v >= want) return;
    __builtin_amdgcn_s_sleep(1);
  }
}

constexpr int K0_GEMV = 108, K0_LN = 128, K0_RQ = 144, K0_RO = 72, K0_RP = 16, K0_Z = 12;
constexpr int K0_GRID = K0_GEMV + K0_LN + K0_RQ + K0_RO + K0_RP + K0_Z;

__global__ __launch_bounds__(256) void k0_prep(const unsigned short* __restrict__ x, const unsigned short* __restrict__ wqkv,
                                               const unsigned short* __restrict__ wphi, const unsigned short* __restrict__ wo,
                                               const unsigned short* __restrict__ c, const unsigned short* __restrict__ wmod,
                                               char* __restrict__ ws) {
  __shared__ __attribute__((aligned(16))) float red[3584];
  const int t = threadIdx.x;
  int b = blockIdx.x;
  if (b < K0_GEMV) {
    const int cc = t & 3, rg = t >> 2;
    u32x4 wv[9];
    unsigned short cv[9];
#pragma unroll
    for (int i = 0; i < 9; ++i) wv[i] = *(const u32x4*)(wmod + (size_t)(rg + 64 * i) * 3456 + b * 32 + cc * 8);
#pragma unroll
    for (int i = 0; i < 9; ++i) cv[i] = c[rg + 64 * i];
    float acc[8] = {0, 0, 0, 0, 0, 0, 0, 0};
#pragma unroll
    for (int i = 0; i < 9; ++i) {
      float cf = bf2f(cv[i]);
      float s = rbf(cf * __builtin_amdgcn_rcpf(1.0f + __expf(-cf)));
#pragma unroll
      for (int j = 0; j < 4; ++j) {
        acc[2 * j] += s * bf_lo(wv[i][j]);
        acc[2 * j + 1] += s * bf_hi(wv[i][j]);
      }
    }
#pragma unroll
    for (int j = 0; j < 8; ++j) red[rg * 33 + cc * 8 + j] = acc[j];
    __syncthreads();
    {
      const int col = t & 31, part = t >> 5;
      float s = 0;
#pragma unroll
      for (int i = 0; i < 8; ++i) s += red[(part * 8 + i) * 33 + col];
      red[64 * 33 + part * 32 + col] = s;
    }
    __syncthreads();
    if (t < 32) {
      float s = 0;
#pragma unroll
      for (int i = 0; i < 8; ++i) s += red[64 * 33 + i * 32 + t];
      const int idx = b * 32 + t, sec = idx / 576;
      float v = rbf(s);
      if (sec == 1 || sec == 4) v = rbf(1.0f + v);
      ((float*)(ws + WS_MODF))[idx] = v;
      ((unsigned short*)(ws + WS_MODH))[idx] = (unsigned short)(__float_as_uint(v) >> 16);
    }
    return;
  }
  b -= K0_GEMV;
  if (b < K0_LN) {
    const int l = t & 63, w = t >> 6, sub = l & 15, tok = w * 4 + (l >> 4);
    u32x2 xv[9];
    const unsigned short* xr = x + (size_t)(b * 16 + tok) * 576 + sub * 4;
#pragma unroll
    for (int i = 0; i < 9; ++i) xv[i] = *(const u32x2*)(xr + i * 64);
    float s = 0;
#pragma unroll
    for (int i = 0; i < 9; ++i) s += (bf_lo(xv[i][0]) + bf_hi(xv[i][0])) + (bf_lo(xv[i][1]) + bf_hi(xv[i][1]));
#pragma unroll
    for (int o = 1; o < 16; o <<= 1) s += __shfl_xor(s, o);
    const float mean = s * (1.0f / 576.0f);
    float q = 0;
#pragma unroll
    for (int i = 0; i < 9; ++i)
#pragma unroll
      for (int j = 0; j < 2; ++j) {
        float a = bf_lo(xv[i][j]) - mean, bb = bf_hi(xv[i][j]) - mean;
        q += a * a + bb * bb;
      }
#pragma unroll
    for (int o = 1; o < 16; o <<= 1) q += __shfl_xor(q, o);
    const float var = q * (1.0f / 576.0f);
    const float mb = rbf(mean);
    const float sd = rbf(sqrtf(rbf(rbf(var) + 1e-5f)));
    const float rs = 1.0f / sd;
    char* dst = ws + WS_XN + (size_t)b * 18432 + (sub >> 3) * 1024 + (tok + 16 * ((sub >> 1) & 3)) * 16 + (sub & 1) * 8;
#pragma unroll
    for (int i = 0; i < 9; ++i) {
      u32x2 o = {pack2(rbf(bf_lo(xv[i][0]) - mb) * rs, rbf(bf_hi(xv[i][0]) - mb) * rs),
                 pack2(rbf(bf_lo(xv[i][1]) - mb) * rs, rbf(bf_hi(xv[i][1]) - mb) * rs)};
      st8<false>(dst + i * 2048, o);
    }
    return;
  }
  b -= K0_LN;
  char* img = (char*)red;
  const int ln = t & 63, wv = t >> 6, fn = ln & 15, fg = ln >> 4;
  auto gather8 = [&](const char* base, int stride, bool valid) {
    unsigned short v[8];
#pragma unroll
    for (int j = 0; j < 8; ++j) v[j] = *(const unsigned short*)(base + j * stride);
    u32x4 o = {v[0] | ((unsigned)v[1] << 16), v[2] | ((unsigned)v[3] << 16), v[4] | ((unsigned)v[5] << 16), v[6] | ((unsigned)v[7] << 16)};
    return valid ? o : (u32x4){0, 0, 0, 0};
  };
  if (b < K0_RQ) {
    const int ks = b >> 3, grp = b & 7;
    u32x4 v[4];
#pragma unroll
    for (int i = 0; i < 4; ++i) {
      const int id = min(t + 256 * i, 863), row = id / 27, r = id % 27, seg = r / 9, c = r % 9;
      v[i] = *(const u32x4*)((const char*)wqkv + (size_t)(ks * 32 + row) * 3456 + seg * 1152 + grp * 144 + c * 16);
    }
#pragma unroll
    for (int i = 0; i < 4; ++i) {
      const int id = min(t + 256 * i, 863), row = id / 27, r = id % 27;
      *(u32x4*)(img + row * 448 + r * 16) = v[i];
    }
    __syncthreads();
#pragma unroll
    for (int i = 0; i < 4; ++i) {
      const int pl = wv & 1, nt = (wv >> 1) + 2 * i, cidx = nt * 16 + fn;
      if (nt < 7) {
        int col = -1;
        if (cidx < 18) col = 144 + cidx;
        else if (cidx < 20) col = -1;
        else if (cidx < 38) col = 144 + 18 + (cidx - 20);
        else if (cidx < 74) col = 72 + (cidx - 38);
        else if (cidx < 110) col = cidx - 74;
        const u32x4 o = gather8(img + fg * 8 * 448 + (max(col, 0) + 36 * pl) * 2, 448, col >= 0);
        const int pi = (nt < 5) ? (ks * 5 + nt) : (90 + ks * 2 + (nt - 5));
        st16<false>(ws, WS_WQ + ((size_t)(grp * 2 + pl) * 126 + pi) * 1024 + ln * 16, o);
      }
    }
    return;
  }
  b -= K0_RQ;
  if (b < K0_RO) {
    const int ks = b >> 2, qd = b & 3;
    u32x4 v[3];
#pragma unroll
    for (int i = 0; i < 3; ++i) {
      const int id = min(t + 256 * i, 575), row = id / 18, c = id % 18;
      const int kidx = ks * 32 + row, pr = kidx / 36, pos = kidx % 36;
      const int cr = 36 * pr + ((pos < 32) ? (18 * (pos >> 4) + (pos & 15)) : (18 * ((pos - 32) >> 1) + 16 + (pos & 1)));
      v[i] = *(const u32x4*)((const char*)wo + (size_t)cr * 1152 + qd * 288 + c * 16);
    }
#pragma unroll
    for (int i = 0; i < 3; ++i) {
      const int id = min(t + 256 * i, 575), row = id / 18, c = id % 18;
      *(u32x4*)(img + row * 304 + c * 16) = v[i];
    }
    __syncthreads();
#pragma unroll
    for (int i = 0; i < 3; ++i) {
      const int nl = wv + 4 * i;
      if (nl < 9) {
        const u32x4 o = gather8(img + fg * 8 * 304 + (nl * 16 + fn) * 2, 304, true);
        st16<false>(ws, WS_WO + ((size_t)(qd * 9 + nl) * 18 + ks) * 1024 + ln * 16, o);
      }
    }
    return;
  }
  b -= K0_RO;
  if (b < K0_RP) {
    if (t < 162) *(u32x2*)(img + t * 8) = *(const u32x2*)((const char*)wphi + b * 1296 + t * 8);
    __syncthreads();
#pragma unroll
    for (int i = 0; i < 2; ++i) {
      const int f8 = wv * 2 + i, ft = f8 & 1, seg = f8 >> 1;
      const int s0 = 38 + 18 * seg, nt0 = s0 >> 4, f = ft * 16 + fn;
      unsigned short v[8];
#pragma unroll
      for (int j = 0; j < 8; ++j) {
        const int d = 16 * (nt0 + (j >> 2)) + 4 * fg + (j & 3) - s0;
        const unsigned short r = *(const unsigned short*)(img + (((seg & 1) * 18 + min(max(d, 0), 17)) * 18 + min(f, 17)) * 2);
        v[j] = (d >= 0 && d < 18 && f < 18) ? r : (unsigned short)0;
      }
      u32x4 o = {v[0] | ((unsigned)v[1] << 16), v[2] | ((unsigned)v[3] << 16), v[4] | ((unsigned)v[5] << 16), v[6] | ((unsigned)v[7] << 16)};
      st16<false>(ws, WS_WPHI + (size_t)(b * 8 + f8) * 1024 + ln * 16, o);
    }
    return;
  }
  b -= K0_RP;
  {
    u32x4 z = {0, 0, 0, 0};
#pragma unroll
    for (int r = 0; r < 4; ++r) *(u32x4*)(ws + WS_FLAGS + (size_t)b * 16384 + r * 4096 + t * 16) = z;
  }
  return;
}

constexpr int K1_LDS_B = 0, K1_LDS_PHI = 129024, K1_LDS_V = 147456, K1_LDS_MISC = 159744, K1_LDS = 159744 + 64;

struct Feat { unsigned pos01, pos23, neg01, neg23, ex0, ex1; };

__device__ __forceinline__ Feat feature_map(bf16x8 w0, bf16x8 w1, bf16x8 bop, int g) {
  f32x4 z = {0, 0, 0, 0};
  f32x4 t0 = mfma16(w0, bop, z);
  f32x4 t1 = mfma16(w1, bop, z);
  float p0 = rbf(t0[0]), p1 = rbf(t0[1]), p2 = rbf(t0[2]), p3 = rbf(t0[3]);
  float e0 = rbf(t1[0]), e1 = rbf(t1[1]);
  const bool g0 = (g == 0);
  if (!g0) { e0 = p0; e1 = p1; }
  float mx = fmaxf(fmaxf(fmaxf(p0, p1), fmaxf(p2, p3)), fmaxf(e0, e1));
  float mn = fminf(fminf(fminf(p0, p1), fminf(p2, p3)), fminf(e0, e1));
  mx = gmax(mx);
  mn = gmin(mn);
  float a0 = __expf(p0 - mx), a1 = __expf(p1 - mx), a2 = __expf(p2 - mx), a3 = __expf(p3 - mx);
  float b0 = __expf(mn - p0), b1 = __expf(mn - p1), b2 = __expf(mn - p2), b3 = __expf(mn - p3);
  float ae0 = __expf(e0 - mx), ae1 = __expf(e1 - mx), be0 = __expf(mn - e0), be1 = __expf(mn - e1);
  if (!g0) { ae0 = 0; ae1 = 0; be0 = 0; be1 = 0; }
  float sp = gsum((a0 + a1) + (a2 + a3) + (ae0 + ae1));
  float sn = gsum((b0 + b1) + (b2 + b3) + (be0 + be1));
  float rp = __builtin_amdgcn_rcpf(sp), rn = __builtin_amdgcn_rcpf(sn);
  Feat f;
  f.pos01 = pack2(a0 * rp, a1 * rp);
  f.pos23 = pack2(a2 * rp, a3 * rp);
  f.neg01 = pack2(b0 * rn, b1 * rn);
  f.neg23 = pack2(b2 * rn, b3 * rn);
  f.ex0 = pack2(ae0 * rp, ae1 * rp);
  f.ex1 = pack2(be0 * rn, be1 * rn);
  return f;
}

__global__ __launch_bounds__(512, 2) __attribute__((amdgpu_waves_per_eu(2, 2))) void k1_attn(const unsigned short* __restrict__ x, const unsigned short* __restrict__ wd, char* __restrict__ ws) {
  extern __shared__ __attribute__((aligned(16))) char smem[];
  const int b = blockIdx.x, xcd = b & 7, bi = b >> 3;
  const int T = (xcd >> 1) * 4 + (bi & 3), p = (xcd & 1) * 8 + (bi >> 2);
  const int w = __builtin_amdgcn_readfirstlane(threadIdx.x >> 6), l = threadIdx.x & 63;
  const int tok = l & 15, g = l >> 4;
  if (threadIdx.x < 2) *(LAS unsigned*)(smem + K1_LDS_MISC + threadIdx.x * 4) = 0;

  const int mt = T * 8 + w;
  const u32x4 dW = mk_desc(ws, 0x7fffffffu);
  u32x4 mv = bload16(dW, (int)WS_MODF + (threadIdx.x < 288 ? threadIdx.x : 0) * 16, 0);
  u32x4 av[18];
#pragma unroll
  for (int ks = 0; ks < 18; ++ks) av[ks] = bload16(dW, l * 16, (int)WS_XN + mt * 18432 + ks * 1024);
  asm volatile("s_waitcnt vmcnt(18)" : "+v"(mv) :: "memory");
  if (threadIdx.x < 288) *(u32x4*)(smem + K1_LDS_PHI + threadIdx.x * 16) = mv;
  RAW_BAR();
  bf16x8 a[18];
  {
    const char* mp = smem + K1_LDS_PHI + g * 32;
    f32x4 mvv[2][4];
#pragma unroll
    for (int i = 0; i < 4; ++i) mvv[0][i] = *(const f32x4*)(mp + (i >> 1) * 2304 + (i & 1) * 16);
#pragma unroll
    for (int ks = 0; ks < 18; ++ks) {
      if (ks == 0) asm volatile("s_waitcnt vmcnt(12)" : "+v"(av[0]), "+v"(av[1]), "+v"(av[2]), "+v"(av[3]), "+v"(av[4]), "+v"(av[5]) :: "memory");
      if (ks == 6) asm volatile("s_waitcnt vmcnt(12)" : "+v"(av[6]), "+v"(av[7]), "+v"(av[8]), "+v"(av[9]), "+v"(av[10]), "+v"(av[11]) :: "memory");
      if (ks == 12) asm volatile("s_waitcnt vmcnt(12)" : "+v"(av[12]), "+v"(av[13]), "+v"(av[14]), "+v"(av[15]), "+v"(av[16]), "+v"(av[17]) :: "memory");
      if (ks < 17) {
#pragma unroll
        for (int i = 0; i < 4; ++i) mvv[(ks + 1) & 1][i] = *(const f32x4*)(mp + (ks + 1) * 128 + (i >> 1) * 2304 + (i & 1) * 16);
      }
      __builtin_amdgcn_sched_barrier(0);
      const f32x4 s0 = mvv[ks & 1][0], s1 = mvv[ks & 1][1], c0 = mvv[ks & 1][2], c1 = mvv[ks & 1][3];
      u32x4 o;
      unsigned m0 = pack2(bf_lo(av[ks][0]) * c0[0], bf_hi(av[ks][0]) * c0[1]), m1 = pack2(bf_lo(av[ks][1]) * c0[2], bf_hi(av[ks][1]) * c0[3]);
      unsigned m2 = pack2(bf_lo(av[ks][2]) * c1[0], bf_hi(av[ks][2]) * c1[1]), m3 = pack2(bf_lo(av[ks][3]) * c1[2], bf_hi(av[ks][3]) * c1[3]);
      o[0] = pack2(bf_lo(m0) + s0[0], bf_hi(m0) + s0[1]);
      o[1] = pack2(bf_lo(m1) + s0[2], bf_hi(m1) + s0[3]);
      o[2] = pack2(bf_lo(m2) + s1[0], bf_hi(m2) + s1[1]);
      o[3] = pack2(bf_lo(m3) + s1[2], bf_hi(m3) + s1[3]);
      a[ks] = __builtin_bit_cast(bf16x8, o);
      __builtin_amdgcn_sched_barrier(0);
      if (ks < 16) {
        const int fr = min(w + 8 * ks, 125);
        blds16(dW, lds_addr_of(smem) + K1_LDS_B + fr * 1024, l * 16, (int)WS_WQ + p * 129024 + fr * 1024);
      }
    }
  }
  u32x4 wpv[8];
#pragma unroll
  for (int i = 0; i < 8; ++i) wpv[i] = bload16(dW, l * 16, (int)WS_WPHI + (p * 8 + i) * 1024);
  f32x4 acc[7];
#pragma unroll
  for (int nt = 0; nt < 7; ++nt) acc[nt] = (f32x4){0, 0, 0, 0};
  const char* bp = smem + K1_LDS_B + l * 16;
  WAIT_V(12);
  RAW_BAR();
  u32x4 pf[4];
  {
    const u32x4 dD = mk_desc(wd, 4608u * 1152u);
    const int ub = min(b, 191) * 3;
#pragma unroll
    for (int i = 0; i < 4; ++i) {
      const int pc = min(w + 8 * i, 26), u = ub + pc / 9, j = pc % 9;
      const int idx = j * 64 + l, pos = idx / 18, c = idx % 18;
      pf[i] = bload16(dD, pos * 1152 + c * 16, (u % 144) * 36864 + (u / 144) * 288);
    }
  }
  {
    bf16x8 bfr[2][5];
#pragma unroll
    for (int k2 = 0; k2 < 2; ++k2)
#pragma unroll
      for (int nt = 0; nt < 5; ++nt) bfr[k2][nt] = *(const bf16x8*)(bp + (k2 * 5 + nt) * 1024);
    __builtin_amdgcn_sched_barrier(0);
#pragma unroll
    for (int ks = 0; ks < 18; ++ks) {
#pragma unroll
      for (int nt = 0; nt < 5; ++nt) {
        acc[nt] = mfma16(bfr[ks & 1][nt], a[ks], acc[nt]);
        if (ks < 16) bfr[ks & 1][nt] = *(const bf16x8*)(bp + ((ks + 2) * 5 + nt) * 1024);
        __builtin_amdgcn_sched_barrier(0);
      }
    }
  }
  asm volatile("s_waitcnt vmcnt(0)" : "+v"(wpv[0]), "+v"(wpv[1]), "+v"(wpv[2]), "+v"(wpv[3]), "+v"(wpv[4]), "+v"(wpv[5]), "+v"(wpv[6]), "+v"(wpv[7]), "+v"(pf[0]), "+v"(pf[1]), "+v"(pf[2]), "+v"(pf[3]) :: "memory");
  bf16x8 wph[4][2];
#pragma unroll
  for (int s = 0; s < 4; ++s)
#pragma unroll
    for (int ft = 0; ft < 2; ++ft) wph[s][ft] = __builtin_bit_cast(bf16x8, wpv[s * 2 + ft]);
  unsigned pk[7][2];
#pragma unroll
  for (int nt = 0; nt < 5; ++nt) {
    pk[nt][0] = pack2(acc[nt][0], acc[nt][1]);
    pk[nt][1] = pack2(acc[nt][2], acc[nt][3]);
  }
  const int tl = w * 16 + tok;
#pragma unroll
  for (int nt = 0; nt < 3; ++nt) *(u32x2*)(smem + K1_LDS_V + tl * 96 + nt * 32 + g * 8) = (u32x2){pk[nt][0], pk[nt][1]};
#pragma unroll
  for (int hh = 0; hh < 2; ++hh) {
    const int nt0 = 2 + hh;
    Feat f = feature_map(wph[hh][0], wph[hh][1], mk8(pk[nt0][0], pk[nt0][1], pk[nt0 + 1][0], pk[nt0 + 1][1]), g);
    char* row = smem + K1_LDS_PHI + tl * 144 + hh * 72;
    *(u32x2*)(row + g * 8) = (u32x2){f.pos01, f.pos23};
    *(u32x2*)(row + 32 + g * 8) = (u32x2){f.neg01, f.neg23};
    if (g == 0) *(u32x2*)(row + 64) = (u32x2){f.ex0, f.ex1};
  }
  __syncthreads();

  const __amdgpu_buffer_rsrc_t kv_rsrc = __builtin_amdgcn_make_buffer_rsrc(ws + WS_KVP + (size_t)p * 16 * 5504, 0, 16 * 5504, 0x00020000);
  const int hw = w & 1, tau = w >> 1;
  const bool vrow = (tau < 2) || (g == 0);
  const unsigned kvo0 = (unsigned)(((w < 4) ? w * 1216 : 4864 + (w - 4) * 304) + l * 16);
  const unsigned kvo1 = (unsigned)(((w < 4) ? w * 1216 + 1024 : 4864 + (w - 4) * 304 + 256) + (g * 3 + tok) * 16);
  gu32* cnt = (gu32*)(ws + WS_FLAGS + OFF_CNT1 + p * 256);
  if (w < 6) {
    const int q = (l >> 2) & 3, pp = l & 3;
    f32x4 d0 = {0, 0, 0, 0}, d1 = {0, 0, 0, 0};
#pragma unroll
    for (int k4 = 0; k4 < 4; ++k4) {
      const int tk = k4 * 32 + g * 8 + q;
      const char* pa = smem + K1_LDS_PHI + tk * 144 + hw * 72 + (16 * tau + 4 * pp) * 2;
      bf16x8 af = cat44(tr_read(pa), tr_read(pa + 4 * 144));
      const char* pv = smem + K1_LDS_V + tk * 96 + (20 * hw + 4 * pp) * 2;
      bf16x8 v0 = cat44(tr_read(pv), tr_read(pv + 4 * 96));
      bf16x8 v1 = cat44(tr_read(pv + 32), tr_read(pv + 32 + 4 * 96));
      if (tok == 2) v1 = mk8(0x3F803F80u, 0x3F803F80u, 0x3F803F80u, 0x3F803F80u);
      d0 = mfma16(af, v0, d0);
      d1 = mfma16(af, v1, d1);
    }
    if (vrow) __builtin_amdgcn_raw_buffer_store_b128(__builtin_bit_cast(u32x4, d0), kv_rsrc, kvo0 + (unsigned)(T * 5504), 0, 16);
    if (vrow && tok < 3) __builtin_amdgcn_raw_buffer_store_b128(__builtin_bit_cast(u32x4, d1), kv_rsrc, kvo1 + (unsigned)(T * 5504), 0, 16);
    WAIT_V(0);
    if (l == 0) {
      unsigned old = __hip_atomic_fetch_add((LAS unsigned*)(smem + K1_LDS_MISC), 1u, __ATOMIC_RELAXED, __HIP_MEMORY_SCOPE_WORKGROUP);
      if (old == 5) __hip_atomic_fetch_add(cnt, 1u, RLX_AGENT);
    }
  }
  const int cg = p & 3, tt = T * 4 + (p >> 2);
  bf16x8 bw[18], bx[3];
  const char* wsrc = ws + WS_WO + (size_t)(cg * 9 + w) * 18432 + l * 16;
  const char* wsrc8 = ws + WS_WO + (size_t)(cg * 9 + 8) * 18432 + l * 16;
  {
    bf16x8 bfr[4][2];
#pragma unroll
    for (int k4 = 0; k4 < 4; ++k4)
#pragma unroll
      for (int nt = 0; nt < 2; ++nt) bfr[k4][nt] = *(const bf16x8*)(bp + (90 + k4 * 2 + nt) * 1024);
    __builtin_amdgcn_sched_barrier(0);
#pragma unroll
    for (int ks = 0; ks < 18; ++ks) {
#pragma unroll
      for (int nt = 0; nt < 2; ++nt) {
        acc[5 + nt] = mfma16(bfr[ks & 3][nt], a[ks], acc[5 + nt]);
        if (ks < 14) bfr[ks & 3][nt] = *(const bf16x8*)(bp + (90 + (ks + 4) * 2 + nt) * 1024);
        __builtin_amdgcn_sched_barrier(0);
      }
      bw[ks] = *(const bf16x8*)(wsrc + ks * 1024);
      if (ks < 3) bx[ks] = *(const bf16x8*)(wsrc8 + min(w + 8 * ks, 17) * 1024);
      __builtin_amdgcn_sched_barrier(0);
    }
#pragma unroll
    for (int nt = 5; nt < 7; ++nt) {
      pk[nt][0] = pack2(acc[nt][0], acc[nt][1]);
      pk[nt][1] = pack2(acc[nt][2], acc[nt][3]);
    }
  }
  bf16x8 qb1[2], qb2[2];
#pragma unroll
  for (int hh = 0; hh < 2; ++hh) {
    const int nt0 = 4 + hh;
    Feat f = feature_map(wph[2 + hh][0], wph[2 + hh][1], mk8(pk[nt0][0], pk[nt0][1], pk[nt0 + 1][0], pk[nt0 + 1][1]), g);
    qb1[hh] = mk8(f.pos01, f.pos23, f.neg01, f.neg23);
    qb2[hh] = mk8(f.ex0, f.ex1, 0u, 0u);
  }
  if (w == 7) wait_ge(cnt, 16u);
  __builtin_amdgcn_fence(__ATOMIC_ACQUIRE, "wavefront");
  __syncthreads();
  if (w < 6) {
    const int t = threadIdx.x;
    u32x4 r0[16];
    const unsigned o0 = (t < 342) ? (unsigned)(t * 16) : 0x7ffffff0u;
#pragma unroll
    for (int i = 0; i < 16; ++i) r0[i] = __builtin_amdgcn_raw_buffer_load_b128(kv_rsrc, o0, i * 5504, 16);
    f32x4 s0 = {0, 0, 0, 0};
#pragma unroll
    for (int i = 0; i < 16; ++i) s0 += __builtin_bit_cast(f32x4, r0[i]);
    const bool lo = t < 304;
    const int ws_ = lo ? t / 76 : 4 + (t - 304) / 19, r = lo ? t % 76 : (t - 304) % 19, nd0 = lo ? 64 : 16;
    const int dt = (r >= nd0), i1 = r - nd0, lane = dt ? (i1 / 3) * 16 + i1 % 3 : r;
    const int tau_ = ws_ >> 1;
    if (t < 342)
      *(u32x2*)(smem + ((tau_ < 2) ? tau_ * 8 : 4096) + ((ws_ & 1) * 2 + dt) * 1024 + lane * 16) = (u32x2){pack2(s0[0], s0[1]), pack2(s0[2], s0[3])};
  }
  __syncthreads();
  {
    char* obase = ws + WS_O + (size_t)mt * 18432 + tok * 16;
    unsigned tail[2];
#pragma unroll
    for (int hh = 0; hh < 2; ++hh) {
      f32x4 o0 = {0, 0, 0, 0}, o1 = {0, 0, 0, 0};
      o0 = mfma16(*(const bf16x8*)(smem + (hh * 2 + 0) * 1024 + l * 16), qb1[hh], o0);
      o1 = mfma16(*(const bf16x8*)(smem + (hh * 2 + 1) * 1024 + l * 16), qb1[hh], o1);
      o0 = mfma16(*(const bf16x8*)(smem + 4096 + (hh * 2 + 0) * 1024 + l * 16), qb2[hh], o0);
      o1 = mfma16(*(const bf16x8*)(smem + 4096 + (hh * 2 + 1) * 1024 + l * 16), qb2[hh], o1);
      float den = __shfl(o1[2], tok);
      den = rbf(rbf(den) + 1e-6f);
      const float rd = __builtin_amdgcn_rcpf(den);
      const unsigned u0 = pack2(rbf(o0[0]) * rd, rbf(o0[1]) * rd), u1 = pack2(rbf(o0[2]) * rd, rbf(o0[3]) * rd);
      tail[hh] = pack2(rbf(o1[0]) * rd, rbf(o1[1]) * rd);
      const int k = 36 * p + 16 * hh + 4 * g;
      __hip_atomic_store((gu64*)(obase + (k >> 5) * 1024 + ((k >> 3) & 3) * 256 + (k & 7) * 2), (unsigned long long)u0 | ((unsigned long long)u1 << 32), RLX_AGENT);
    }
    if (g == 0) {
      const int k = 36 * p + 32;
      __hip_atomic_store((gu64*)(obase + (k >> 5) * 1024 + ((k >> 3) & 3) * 256 + (k & 7) * 2), (unsigned long long)tail[0] | ((unsigned long long)tail[1] << 32), RLX_AGENT);
    }
  }
  gu32* cntT = (gu32*)(ws + WS_FLAGS + OFF_CNTT + T * 256);
  WAIT_V(0);
  if (l == 0) {
    unsigned old = __hip_atomic_fetch_add((LAS unsigned*)(smem + K1_LDS_MISC + 4), 1u, __ATOMIC_RELAXED, __HIP_MEMORY_SCOPE_WORKGROUP);
    if (old == 7) __hip_atomic_fetch_add(cntT, 1u, RLX_AGENT);
  }
  if (b < 192 && w < 7) {
    const u32x4 dD = mk_desc(wd, 4608u * 1152u);
#pragma unroll
    for (int i = 0; i < 4; ++i) {
      const int pc = min(w + 7 * i, 26), u = b * 3 + pc / 9, j = pc % 9;
      const int idx = j * 64 + l, pos = idx / 18, c = idx % 18;
      const int kr = pos ^ (((pos >> 3) & 1) << 2);
      blds16(dD, lds_addr_of(smem) + 65536 + pc * 1024, kr * 1152 + c * 16, (u % 144) * 36864 + (u / 144) * 288);
    }
  }
  const int n0 = (cg * 9 + w) * 16 + g * 4, n8 = (cg * 9 + 8) * 16 + g * 4;
  u32x2 xr[2], xr8[2];
  const float* modf = (const float*)(ws + WS_MODF);
#pragma unroll
  for (int m = 0; m < 2; ++m) {
    xr[m] = *(const u32x2*)(x + (size_t)(tt * 32 + m * 16 + tok) * 576 + n0);
    xr8[m] = *(const u32x2*)(x + (size_t)(tt * 32 + m * 16 + tok) * 576 + n8);
  }
  const f32x4 ga = *(const f32x4*)(modf + 1152 + n0), sb = *(const f32x4*)(modf + 1728 + n0), c1 = *(const f32x4*)(modf + 2304 + n0);
  const f32x4 ga8 = *(const f32x4*)(modf + 1152 + n8), sb8 = *(const f32x4*)(modf + 1728 + n8), c18 = *(const f32x4*)(modf + 2304 + n8);
  if (w == 7) wait_ge(cntT, 16u);
  __builtin_amdgcn_fence(__ATOMIC_ACQUIRE, "wavefront");
  __syncthreads();
  {
    const __amdgpu_buffer_rsrc_t o_rsrc = __builtin_amdgcn_make_buffer_rsrc(ws + WS_O + (size_t)tt * 36864, 0, 36864, 0x00020000);
    u32x4 t5[5];
#pragma unroll
    for (int i = 0; i < 5; ++i) t5[i] = __builtin_amdgcn_raw_buffer_load_b128(o_rsrc, (unsigned)((w + 8 * i) * 1024 + l * 16), 0, 16);
#pragma unroll
    for (int i = 0; i < 5; ++i) *(u32x4*)(smem + (w + 8 * i) * 1024 + l * 16) = t5[i];
  }
  __syncthreads();
  f32x4 ac[2] = {{0, 0, 0, 0}, {0, 0, 0, 0}};
  {
    f32x4 a8[2] = {{0, 0, 0, 0}, {0, 0, 0, 0}};
#pragma unroll
    for (int i = 0; i < 3; ++i) {
      if (w + 8 * i < 18) {
#pragma unroll
        for (int m = 0; m < 2; ++m) a8[m] = mfma16(bx[i], *(const bf16x8*)(smem + (m * 18 + w + 8 * i) * 1024 + l * 16), a8[m]);
      }
    }
    bf16x8 ar[4][2];
#pragma unroll
    for (int k = 0; k < 4; ++k)
#pragma unroll
      for (int m = 0; m < 2; ++m) ar[k][m] = *(const bf16x8*)(smem + (m * 18 + k) * 1024 + l * 16);
    __builtin_amdgcn_sched_barrier(0);
#pragma unroll
    for (int ks = 0; ks < 18; ++ks) {
#pragma unroll
      for (int m = 0; m < 2; ++m) {
        ac[m] = mfma16(bw[ks], ar[ks & 3][m], ac[m]);
        if (ks + 4 < 18) ar[ks & 3][m] = *(const bf16x8*)(smem + (m * 18 + ks + 4) * 1024 + l * 16);
        __builtin_amdgcn_sched_barrier(0);
      }
    }
#pragma unroll
    for (int m = 0; m < 2; ++m) *(f32x4*)(smem + 40960 + (w * 2 + m) * 1024 + l * 16) = a8[m];
  }
  float* stat = (float*)(smem + 57344);
  float* fin = (float*)(smem + 57344 + 2304);
  auto resid = [&](f32x4 acc_, u32x2 xr_, f32x4 ga_, int slot, int m, int ncol, float (&xo)[4]) {
    float xin[4] = {bf_lo(xr_[0]), bf_hi(xr_[0]), bf_lo(xr_[1]), bf_hi(xr_[1])};
#pragma unroll
    for (int i = 0; i < 4; ++i) xo[i] = rbf(xin[i] + rbf(ga_[i] * rbf(acc_[i])));
    float sm = gsum((xo[0] + xo[1]) + (xo[2] + xo[3]));
    float mu = sm * (1.0f / 16.0f);
    float qq = 0;
#pragma unroll
    for (int i = 0; i < 4; ++i) qq += (xo[i] - mu) * (xo[i] - mu);
    qq = gsum(qq);
    if (g == 0) { stat[(slot * 32 + m * 16 + tok) * 2] = mu; stat[(slot * 32 + m * 16 + tok) * 2 + 1] = qq; }
    st8<true>(ws + WS_X1 + ((size_t)(tt * 32 + m * 16 + tok) * 576 + ncol) * 2, (u32x2){pack2(xo[0], xo[1]), pack2(xo[2], xo[3])});
  };
  float x1[2][4], x18[2][4];
#pragma unroll
  for (int m = 0; m < 2; ++m) resid(ac[m], xr[m], ga, w, m, n0, x1[m]);
  __syncthreads();
  if (w == 0) {
#pragma unroll
    for (int m = 0; m < 2; ++m) {
      f32x4 t = *(const f32x4*)(smem + 40960 + m * 1024 + l * 16);
#pragma unroll
      for (int i = 1; i < 8; ++i) t += *(const f32x4*)(smem + 40960 + (i * 2 + m) * 1024 + l * 16);
      resid(t, xr8[m], ga8, 8, m, n8, x18[m]);
    }
    WAIT_L0();
    const int tk = l & 31;
    float mu = 0;
#pragma unroll
    for (int i = 0; i < 9; ++i) mu += stat[(i * 32 + tk) * 2];
    mu *= (1.0f / 9.0f);
    float m2 = 0;
#pragma unroll
    for (int i = 0; i < 9; ++i) {
      float dm = stat[(i * 32 + tk) * 2] - mu;
      m2 += stat[(i * 32 + tk) * 2 + 1] + 16.0f * dm * dm;
    }
    gu64* gr = (gu64*)(ws + WS_FLAGS + OFF_GRAN2) + (size_t)tt * 256;
    const float mine = (l < 32) ? mu : m2;
    __hip_atomic_store(gr + cg * 64 + l, (1ull << 32) | (unsigned long long)__float_as_uint(mine), RLX_AGENT);
    float val[4];
    for (unsigned spins = 0; spins < kSpinCap; ++spins) {
      bool ok = true;
#pragma unroll
      for (int j = 0; j < 4; ++j) {
        unsigned long long v = __hip_atomic_load(gr + j * 64 + l, RLX_AGENT);
        val[j] = __uint_as_float((unsigned)v);
        ok &= ((v >> 32) == 1ull);
      }
      if (__all(ok)) break;
      __builtin_amdgcn_s_sleep(1);
    }
    float m2v[4];
#pragma unroll
    for (int j = 0; j < 4; ++j) m2v[j] = __shfl(val[j], tk + 32);
    float mean = ((val[0] + val[1]) + (val[2] + val[3])) * 0.25f;
    float M2 = 0;
#pragma unroll
    for (int j = 0; j < 4; ++j) { float dm = val[j] - mean; M2 += m2v[j] + 144.0f * dm * dm; }
    const float var = M2 * (1.0f / 576.0f);
    if (l < 32) {
      fin[tk * 2] = rbf(mean);
      fin[tk * 2 + 1] = 1.0f / rbf(sqrtf(rbf(rbf(var) + 1e-5f)));
    }
  } else if (b < 192) {
    const int q = (l >> 2) & 3, pp = l & 3;
    const int rb0 = 65536 + 288 * (8 * g + q + 4 * (g & 1)) + 8 * pp, rb1 = 65536 + 288 * (8 * g + q + 4 * ((g & 1) ^ 1)) + 8 * pp;
#pragma unroll
    for (int i = 0; i < 4; ++i) {
      const int fr = (w - 1) + 7 * i;
      if (fr < 27) {
        const int uo = (fr / 9) * 9216 + (fr % 9) * 32;
        bf16x8 v = cat44(tr_read(smem + rb0 + uo), tr_read(smem + rb1 + uo));
        st16<true>(ws, WS_WD + ((size_t)b * 27 + fr) * 1024 + l * 16, __builtin_bit_cast(u32x4, v));
      }
    }
  }
  __syncthreads();
  auto norm = [&](const float (&xo)[4], f32x4 c1_, f32x4 sb_, int m, int nt) {
    const float mb = fin[(m * 16 + tok) * 2], rs = fin[(m * 16 + tok) * 2 + 1];
    float h[4];
#pragma unroll
    for (int i = 0; i < 4; ++i) h[i] = rbf(rbf(rbf(xo[i] - mb) * rs) * c1_[i]) + sb_[i];
    char* dst = ws + WS_H2 + ((size_t)(tt * 2 + m) * 18 + (nt >> 1)) * 1024 + (tok + 16 * (2 * (nt & 1) + (g >> 1))) * 16 + (g & 1) * 8;
    st8<true>(dst, (u32x2){pack2(h[0], h[1]), pack2(h[2], h[3])});
  };
#pragma unroll
  for (int m = 0; m < 2; ++m) norm(x1[m], c1, sb, m, cg * 9 + w);
  if (w == 0) {
#pragma unroll
    for (int m = 0; m < 2; ++m) norm(x18[m], c18, sb8, m, cg * 9 + 8);
  }
}

constexpr int K3_STAGE = 34816, K3_LDS = 4 * K3_STAGE;

template <int NT, bool five>
__device__ __forceinline__ void k3_body(char* __restrict__ ws, char* smem, u32x4 dA, u32x4 dG, u32x4 dU, const int (&voff)[5], int w, int l,
                                        int T8, int cgp) {
  const int wm = w & 3, wn = w >> 2;
  const int g = l >> 4, q = (l >> 2) & 3, pp = l & 3;
  const unsigned lb = lds_addr_of(smem) + w * 1024;
  const u32x4 d3 = (w == 0) ? dG : dU;
  auto piece = [&](int ks, int i) {
    const unsigned base = lb + (ks & 3) * K3_STAGE;
    if (i == 0) blds16(dA, base, voff[0], ks * 1024);
    if (i == 1) blds16(dA, base + 8192, voff[1], ks * 1024);
    if (i == 2) blds16(dG, base + 16384, voff[2], ks * 294912);
    if (i == 3) blds16(d3, base + 24576, voff[3], ks * 294912);
    if (i == 4) { if (five) blds16(dU, base + 32768, voff[4], ks * 294912); }
  };
#pragma unroll
  for (int ks = 0; ks < 2; ++ks)
#pragma unroll
    for (int i = 0; i < 5; ++i) piece(ks, i);

  f32x4 accg[4][NT], accu[4][NT];
#pragma unroll
  for (int m = 0; m < 4; ++m)
#pragma unroll
    for (int n = 0; n < NT; ++n) { accg[m][n] = (f32x4){0, 0, 0, 0}; accu[m][n] = (f32x4){0, 0, 0, 0}; }

  const int aoff = wm * 4096 + l * 16;
  const int boff0 = 16384 + 288 * (8 * g + q + 4 * (g & 1)) + 8 * pp + wn * 160;
  const int boff1 = 16384 + 288 * (8 * g + q + 4 * ((g & 1) ^ 1)) + 8 * pp + wn * 160;

  bf16x8 af[4], bg[NT], bu[NT];
  WAIT_V(0);
  RAW_BAR();
#pragma unroll
  for (int m = 0; m < 4; ++m) af[m] = *(const bf16x8*)(smem + aoff + m * 1024);
#pragma unroll
  for (int n = 0; n < NT; ++n) {
    bg[n] = cat44(tr_read(smem + boff0 + n * 32), tr_read(smem + boff1 + n * 32));
    bu[n] = cat44(tr_read(smem + boff0 + 9216 + n * 32), tr_read(smem + boff1 + 9216 + n * 32));
  }
#pragma unroll
  for (int ks = 2; ks < 4; ++ks)
#pragma unroll
    for (int i = 0; i < 5; ++i) piece(ks, i);
  auto step = [&](int ks, int mode) {
    const bool more = (mode != 2);
    const int so = ((ks + 1) & 3) * K3_STAGE;
    const int oa = aoff + so, o0 = boff0 + so, o1 = boff1 + so;
    __builtin_amdgcn_sched_barrier(0);
#pragma unroll
    for (int n = 0; n < NT; ++n) {
#pragma unroll
      for (int m = 0; m < 4; ++m) {
        accg[m][n] = mfma16(bg[n], af[m], accg[m][n]);
        accu[m][n] = mfma16(bu[n], af[m], accu[m][n]);
        if (n == NT - 1 && more) {
          __builtin_amdgcn_sched_barrier(0);
          af[m] = *(const bf16x8*)(smem + oa + m * 1024);
          __builtin_amdgcn_sched_barrier(0);
        }
      }
      __builtin_amdgcn_sched_barrier(0);
      if (n == 1 && mode == 0) {
        if (five) WAIT_V(5); else WAIT_V(4);
        RAW_BAR();
      }
      if (n == 1 && mode == 1) {
        if (ks < 16) {
          if (ks < 15) { if (five) WAIT_V(5); else WAIT_V(4); } else WAIT_V(0);
          RAW_BAR();
        }
      }
      if (more) {
        bg[n] = cat44(tr_read(smem + o0 + n * 32), tr_read(smem + o1 + n * 32));
        bu[n] = cat44(tr_read(smem + o0 + 9216 + n * 32), tr_read(smem + o1 + 9216 + n * 32));
      }
      if (mode == 0) {
        if (n == 1) { piece(ks + 4, 0); piece(ks + 4, 1); }
        if (n == 2) piece(ks + 4, 2);
        if (n == 3) piece(ks + 4, 3);
        if (n == 4) piece(ks + 4, 4);
      }
      __builtin_amdgcn_sched_barrier(0);
    }
  };
#pragma unroll 1
  for (int ks = 0; ks < 14; ++ks) step(ks, 0);
#pragma unroll 1
  for (int ks = 14; ks < 17; ++ks) step(ks, 1);
  int lz = threadIdx.x;
  asm volatile("" : "+v"(lz));
  const unsigned lofs = (unsigned)((lz & 15) * 16 + ((lz >> 5) & 1) * 256 + ((lz >> 4) & 1) * 147456);
  const __amdgpu_buffer_rsrc_t hh_rsrc = __builtin_amdgcn_make_buffer_rsrc(ws + WS_HH, 0, 128 * 144 * 1024, 0x00020000);
  auto epi = [&](int n) {
#pragma unroll
    for (int mp = 0; mp < 2; ++mp) {
      unsigned u[2][2];
#pragma unroll
      for (int mm = 0; mm < 2; ++mm) {
        const int m = mp * 2 + mm;
#pragma unroll
        for (int i = 0; i < 2; ++i) {
          const float k1 = -2.0f * 0.7978845608028654f * 1.4426950408889634f, k2 = k1 * 0.044715f;
          const f32x2 xx = {accg[m][n][2 * i], accg[m][n][2 * i + 1]}, uu = {accu[m][n][2 * i], accu[m][n][2 * i + 1]};
          const f32x2 aa = xx * __builtin_elementwise_fma(xx * xx, (f32x2){k2, k2}, (f32x2){k1, k1});
          const f32x2 dd = (f32x2){__builtin_amdgcn_exp2f(aa[0]), __builtin_amdgcn_exp2f(aa[1])} + (f32x2){1.0f, 1.0f};
          const f32x2 hv = (xx * uu) * (f32x2){__builtin_amdgcn_rcpf(dd[0]), __builtin_amdgcn_rcpf(dd[1])};
          u[mm][i] = pack2(hv[0], hv[1]);
        }
      }
      const auto s0 = __builtin_amdgcn_permlane16_swap(u[0][0], u[1][0], false, false);
      const auto s1 = __builtin_amdgcn_permlane16_swap(u[0][1], u[1][1], false, false);
      const int ntg = cgp * 9 + wn * 5 + n;
      __builtin_amdgcn_raw_buffer_store_b128((u32x4){s0[0], s1[0], s0[1], s1[1]}, hh_rsrc,
                                             lofs + (unsigned)(((T8 * 16 + wm * 4 + mp * 2) * 144 + (ntg >> 1)) * 1024 + (ntg & 1) * 512), 0, 16);
      asm volatile("s_nop 1");
      __builtin_amdgcn_sched_barrier(0);
    }
  };
#pragma unroll
  for (int n = 0; n < NT; ++n) {
#pragma unroll
    for (int m = 0; m < 4; ++m) {
      accg[m][n] = mfma16(bg[n], af[m], accg[m][n]);
      accu[m][n] = mfma16(bu[n], af[m], accu[m][n]);
    }
    __builtin_amdgcn_sched_barrier(0);
    if (n > 0) epi(n - 1);
  }
  epi(NT - 1);
}

__global__ __launch_bounds__(512, 2) __attribute__((amdgpu_waves_per_eu(2, 2))) void k3_gateup(const unsigned short* __restrict__ wg, const unsigned short* __restrict__ wu,
                                                    char* __restrict__ ws) {
  extern __shared__ __attribute__((aligned(16))) char smem[];
  const int b = blockIdx.x, xcd = b & 7, bi = b >> 3;
  const int T8 = (xcd >> 2) * 4 + (bi & 3), cgp = (xcd & 3) * 8 + (bi >> 2);
  const int w = __builtin_amdgcn_readfirstlane(threadIdx.x >> 6), l = threadIdx.x & 63;
  int voff[5];
#pragma unroll
  for (int i = 0; i < 5; ++i) {
    const int s = i * 8 + w;
    if (i < 2) {
      voff[i] = (T8 * 16 + s) * 18432 + l * 16;
    } else {
      const int sj = (s < 34) ? s - 16 : 17;
      const int j = (sj < 9) ? sj : sj - 9;
      const int idx = j * 64 + l, pos = idx / 18, c = idx % 18;
      const int kr = pos ^ (((pos >> 3) & 1) << 2);
      voff[i] = kr * 9216 + cgp * 288 + c * 16;
    }
  }
  const u32x4 dA = mk_desc(ws + WS_H2, (unsigned)FRAG_ACT), dG = mk_desc(wg, 576u * 9216u), dU = mk_desc(wu, 576u * 9216u);
  if (w < 2) k3_body<5, true>(ws, smem, dA, dG, dU, voff, w, l, T8, cgp);
  else if (w < 4) k3_body<5, false>(ws, smem, dA, dG, dU, voff, w, l, T8, cgp);
  else k3_body<4, false>(ws, smem, dA, dG, dU, voff, w, l, T8, cgp);
}

constexpr int K4_STAGE = 18432, K4_LDS = 8 * K4_STAGE + 64;

__global__ __launch_bounds__(512, 2) __attribute__((amdgpu_waves_per_eu(2, 2))) void k4_down(char* __restrict__ ws,
                                                  unsigned short* __restrict__ out) {
  extern __shared__ __attribute__((aligned(16))) char smem[];
  const int b = blockIdx.x, xcd = b & 7, bi = b >> 3;
  const int js = xcd & 3, T = (xcd >> 2) * 8 + (bi & 7), cg = bi >> 3;
  const int w = __builtin_amdgcn_readfirstlane(threadIdx.x >> 6), l = threadIdx.x & 63;
  const int wm = w & 3, kk = w >> 2;
  const int tok = l & 15, g = l >> 4;
  if (threadIdx.x == 0) *(LAS unsigned*)(smem + 8 * K4_STAGE) = 0;

  const u32x4 rA = mk_desc(ws + WS_HH, 128u * 144u * 1024u);
  const u32x4 rB = mk_desc(ws + WS_WD, 5184u * 1024u);
  int voffB[3];
#pragma unroll
  for (int i = 0; i < 3; ++i) voffB[i] = ((cg * 144 + js * 36) * 9 + min(i * 8 + w, 17)) * 1024 + l * 16;
  const int voffA = ((T * 8 + 2 * wm) * 144 + js * 36 + kk) * 1024 + l * 16;
  const bool three = (w < 2);
  const unsigned lbase = lds_addr_of(smem) + w * 1024;
  u32x4 ar[6][2];
#define K4_A(st) do { ar[(st) % 6][0] = bload16(rA, voffA, (st) * 2048); ar[(st) % 6][1] = bload16(rA, voffA, (st) * 2048 + 147456); } while (0)
#define K4_B(st) do { blds16(rB, lbase + ((st) & 7) * K4_STAGE, voffB[0], (st) * 18432); blds16(rB, lbase + ((st) & 7) * K4_STAGE + 8192, voffB[1], (st) * 18432); \
    if (three) blds16(rB, lbase + ((st) & 7) * K4_STAGE + 16384, voffB[2], (st) * 18432); } while (0)

  f32x4 acc[2][9];
#pragma unroll
  for (int m = 0; m < 2; ++m)
#pragma unroll
    for (int n = 0; n < 9; ++n) acc[m][n] = (f32x4){0, 0, 0, 0};

  const int boff0 = kk * 9216 + l * 16;
  bf16x8 bw[9];
#define K4_WAIT(n3_, n2_, ...) asm volatile("s_waitcnt vmcnt(" #n2_ ")" : __VA_ARGS__ :: "memory")
#define K4_GROUP(st, n3) do { _Pragma("unroll") for (int nn = 0; nn < 3; ++nn) _Pragma("unroll") for (int m = 0; m < 2; ++m) \
      acc[m][(n3) * 3 + nn] = mfma16(bw[(n3) * 3 + nn], __builtin_bit_cast(bf16x8, ar[(st) % 6][m]), acc[m][(n3) * 3 + nn]); __builtin_amdgcn_sched_barrier(0); } while (0)
#define K4_REFILL(st, n3) do { _Pragma("unroll") for (int nn = 0; nn < 3; ++nn) \
      bw[(n3) * 3 + nn] = K4_RD(((n3) * 3 + nn)); __builtin_amdgcn_sched_barrier(0); } while (0)
#define K4_RD(n) (*(const bf16x8*)(smem + o0 + (n) * 1024))
  int o0;
  K4_A(0); __builtin_amdgcn_sched_barrier(0);
  K4_B(0); __builtin_amdgcn_sched_barrier(0);
  K4_A(1); __builtin_amdgcn_sched_barrier(0);
  K4_B(1); __builtin_amdgcn_sched_barrier(0);
  K4_A(2); __builtin_amdgcn_sched_barrier(0);
  K4_B(2); __builtin_amdgcn_sched_barrier(0);
  K4_WAIT(0, 8, "+v"(ar[0][0]), "+v"(ar[0][1]));
  RAW_BAR();
#pragma unroll
  for (int n = 0; n < 9; ++n) bw[n] = *(const bf16x8*)(smem + boff0 + n * 1024);
  o0 = boff0; asm volatile("" : "+v"(o0)); o0 += 18432; __builtin_amdgcn_sched_barrier(0);
  K4_GROUP(0, 0);
  K4_WAIT(0, 4, "+v"(ar[1][0]), "+v"(ar[1][1]));
  RAW_BAR();
  K4_REFILL(0, 0);
  K4_A(3); __builtin_amdgcn_sched_barrier(0);
  K4_B(3); __builtin_amdgcn_sched_barrier(0);
  K4_GROUP(0, 1);
  K4_REFILL(0, 1);
  K4_A(4); __builtin_amdgcn_sched_barrier(0);
  K4_B(4); __builtin_amdgcn_sched_barrier(0);
  K4_GROUP(0, 2);
  K4_REFILL(0, 2);
  K4_A(5); __builtin_amdgcn_sched_barrier(0);
  K4_B(5); __builtin_amdgcn_sched_barrier(0);
  K4_A(6); __builtin_amdgcn_sched_barrier(0);
  o0 = boff0; asm volatile("" : "+v"(o0)); o0 += 36864; __builtin_amdgcn_sched_barrier(0);
  K4_GROUP(1, 0);
  K4_WAIT(0, 14, "+v"(ar[2][0]), "+v"(ar[2][1]));
  RAW_BAR();
  K4_REFILL(1, 0);
  K4_B(6); __builtin_amdgcn_sched_barrier(0);
  K4_GROUP(1, 1);
  K4_REFILL(1, 1);
  K4_B(7); __builtin_amdgcn_sched_barrier(0);
  K4_GROUP(1, 2);
  K4_REFILL(1, 2);
  K4_A(7); __builtin_amdgcn_sched_barrier(0);
  o0 = boff0; asm volatile("" : "+v"(o0)); o0 += 55296; __builtin_amdgcn_sched_barrier(0);
  K4_GROUP(2, 0);
  K4_WAIT(0, 12, "+v"(ar[3][0]), "+v"(ar[3][1]), "+v"(ar[4][0]), "+v"(ar[4][1]));
  RAW_BAR();
  K4_REFILL(2, 0);
  K4_B(8); __builtin_amdgcn_sched_barrier(0);
  K4_GROUP(2, 1);
  K4_REFILL(2, 1);
  K4_B(9); __builtin_amdgcn_sched_barrier(0);
  K4_B(10); __builtin_amdgcn_sched_barrier(0);
  K4_GROUP(2, 2);
  K4_REFILL(2, 2);
  K4_A(8); __builtin_amdgcn_sched_barrier(0);
  o0 = boff0; asm volatile("" : "+v"(o0)); o0 += 73728; __builtin_amdgcn_sched_barrier(0);
  K4_GROUP(3, 0);
  K4_REFILL(3, 0);
  K4_GROUP(3, 1);
  K4_REFILL(3, 1);
  K4_GROUP(3, 2);
  K4_REFILL(3, 2);
  K4_A(9); __builtin_amdgcn_sched_barrier(0);
  o0 = boff0; asm volatile("" : "+v"(o0)); o0 += 92160; __builtin_amdgcn_sched_barrier(0);
  K4_GROUP(4, 0);
  K4_WAIT(0, 14, "+v"(ar[5][0]), "+v"(ar[5][1]), "+v"(ar[0][0]), "+v"(ar[0][1]));
  RAW_BAR();
  K4_REFILL(4, 0);
  K4_B(11); __builtin_amdgcn_sched_barrier(0);
  K4_GROUP(4, 1);
  K4_REFILL(4, 1);
  K4_B(12); __builtin_amdgcn_sched_barrier(0);
  K4_GROUP(4, 2);
  K4_REFILL(4, 2);
  K4_A(10); __builtin_amdgcn_sched_barrier(0);
  o0 = boff0; asm volatile("" : "+v"(o0)); o0 += 110592; __builtin_amdgcn_sched_barrier(0);
  K4_GROUP(5, 0);
  K4_REFILL(5, 0);
  K4_GROUP(5, 1);
  K4_REFILL(5, 1);
  K4_GROUP(5, 2);
  K4_REFILL(5, 2);
  K4_A(11); __builtin_amdgcn_sched_barrier(0);
  o0 = boff0; asm volatile("" : "+v"(o0)); o0 += 129024; __builtin_amdgcn_sched_barrier(0);
  K4_GROUP(6, 0);
  K4_WAIT(0, 10, "+v"(ar[1][0]), "+v"(ar[1][1]), "+v"(ar[2][0]), "+v"(ar[2][1]));
  RAW_BAR();
  K4_REFILL(6, 0);
  K4_B(13); __builtin_amdgcn_sched_barrier(0);
  K4_GROUP(6, 1);
  K4_REFILL(6, 1);
  K4_B(14); __builtin_amdgcn_sched_barrier(0);
  K4_GROUP(6, 2);
  K4_REFILL(6, 2);
  K4_A(12); __builtin_amdgcn_sched_barrier(0);
  o0 = boff0; asm volatile("" : "+v"(o0)); o0 += 0; __builtin_amdgcn_sched_barrier(0);
  K4_GROUP(7, 0);
  K4_REFILL(7, 0);
  K4_GROUP(7, 1);
  K4_REFILL(7, 1);
  K4_GROUP(7, 2);
  K4_REFILL(7, 2);
  K4_A(13); __builtin_amdgcn_sched_barrier(0);
  o0 = boff0; asm volatile("" : "+v"(o0)); o0 += 18432; __builtin_amdgcn_sched_barrier(0);
  K4_GROUP(8, 0);
  K4_WAIT(0, 10, "+v"(ar[3][0]), "+v"(ar[3][1]), "+v"(ar[4][0]), "+v"(ar[4][1]));
  RAW_BAR();
  K4_REFILL(8, 0);
  K4_B(15); __builtin_amdgcn_sched_barrier(0);
  K4_GROUP(8, 1);
  K4_REFILL(8, 1);
  K4_B(16); __builtin_amdgcn_sched_barrier(0);
  K4_GROUP(8, 2);
  K4_REFILL(8, 2);
  K4_A(14); __builtin_amdgcn_sched_barrier(0);
  o0 = boff0; asm volatile("" : "+v"(o0)); o0 += 36864; __builtin_amdgcn_sched_barrier(0);
  K4_GROUP(9, 0);
  K4_REFILL(9, 0);
  K4_GROUP(9, 1);
  K4_REFILL(9, 1);
  K4_GROUP(9, 2);
  K4_REFILL(9, 2);
  K4_A(15); __builtin_amdgcn_sched_barrier(0);
  o0 = boff0; asm volatile("" : "+v"(o0)); o0 += 55296; __builtin_amdgcn_sched_barrier(0);
  K4_GROUP(10, 0);
  K4_WAIT(0, 10, "+v"(ar[5][0]), "+v"(ar[5][1]), "+v"(ar[0][0]), "+v"(ar[0][1]));
  RAW_BAR();
  K4_REFILL(10, 0);
  K4_B(17); __builtin_amdgcn_sched_barrier(0);
  K4_GROUP(10, 1);
  K4_REFILL(10, 1);
  K4_GROUP(10, 2);
  K4_REFILL(10, 2);
  K4_A(16); __builtin_amdgcn_sched_barrier(0);
  o0 = boff0; asm volatile("" : "+v"(o0)); o0 += 73728; __builtin_amdgcn_sched_barrier(0);
  K4_GROUP(11, 0);
  K4_REFILL(11, 0);
  K4_GROUP(11, 1);
  K4_REFILL(11, 1);
  K4_GROUP(11, 2);
  K4_REFILL(11, 2);
  K4_A(17); __builtin_amdgcn_sched_barrier(0);
  o0 = boff0; asm volatile("" : "+v"(o0)); o0 += 92160; __builtin_amdgcn_sched_barrier(0);
  K4_GROUP(12, 0);
  K4_WAIT(0, 8, "+v"(ar[1][0]), "+v"(ar[1][1]), "+v"(ar[2][0]), "+v"(ar[2][1]));
  RAW_BAR();
  K4_REFILL(12, 0);
  K4_GROUP(12, 1);
  K4_REFILL(12, 1);
  K4_GROUP(12, 2);
  K4_REFILL(12, 2);
  o0 = boff0; asm volatile("" : "+v"(o0)); o0 += 110592; __builtin_amdgcn_sched_barrier(0);
  K4_GROUP(13, 0);
  K4_REFILL(13, 0);
  K4_GROUP(13, 1);
  K4_REFILL(13, 1);
  K4_GROUP(13, 2);
  K4_REFILL(13, 2);
  o0 = boff0; asm volatile("" : "+v"(o0)); o0 += 129024; __builtin_amdgcn_sched_barrier(0);
  K4_GROUP(14, 0);
  K4_WAIT(0, 6, "+v"(ar[3][0]), "+v"(ar[3][1]));
  RAW_BAR();
  K4_REFILL(14, 0);
  K4_GROUP(14, 1);
  K4_REFILL(14, 1);
  K4_GROUP(14, 2);
  K4_REFILL(14, 2);
  o0 = boff0; asm volatile("" : "+v"(o0)); o0 += 0; __builtin_amdgcn_sched_barrier(0);
  K4_GROUP(15, 0);
  K4_WAIT(0, 2, "+v"(ar[4][0]), "+v"(ar[4][1]));
  RAW_BAR();
  K4_REFILL(15, 0);
  K4_GROUP(15, 1);
  K4_REFILL(15, 1);
  K4_GROUP(15, 2);
  K4_REFILL(15, 2);
  o0 = boff0; asm volatile("" : "+v"(o0)); o0 += 18432; __builtin_amdgcn_sched_barrier(0);
  K4_GROUP(16, 0);
  K4_WAIT(0, 0, "+v"(ar[5][0]), "+v"(ar[5][1]));
  RAW_BAR();
  K4_REFILL(16, 0);
  K4_GROUP(16, 1);
  K4_REFILL(16, 1);
  K4_GROUP(16, 2);
  K4_REFILL(16, 2);
  K4_GROUP(17, 0);
  K4_GROUP(17, 1);
  K4_GROUP(17, 2);
  __syncthreads();
#pragma unroll
  for (int n = 0; n < 9; ++n) *(f32x4*)(smem + (w * 9 + n) * 1024 + l * 16) = kk ? acc[0][n] : acc[1][n];
  __syncthreads();
  f32x4 r[9];
#pragma unroll
  for (int n = 0; n < 9; ++n) {
    f32x4 o = *(const f32x4*)(smem + ((w ^ 4) * 9 + n) * 1024 + l * 16);
    r[n] = kk ? (o + acc[1][n]) : (acc[0][n] + o);
  }
  const int tile = T * 4 + cg;
  const __amdgpu_buffer_rsrc_t sl = __builtin_amdgcn_make_buffer_rsrc(ws + WS_SLAB + (size_t)tile * 294912, 0, 294912, 0x00020000);
  const unsigned soff = (unsigned)(w * 9216 + l * 16);
#pragma unroll
  for (int d = 0; d < 4; ++d) {
    const int n = (d == 0) ? 0 : (2 * d + 1);
    if (d != js) {
      u32x4 v = {pack2(r[n][0], r[n][1]), pack2(r[n][2], r[n][3]), pack2(r[n + 1][0], r[n + 1][1]), pack2(r[n + 1][2], r[n + 1][3])};
      __builtin_amdgcn_raw_buffer_store_b128(v, sl, soff + js * 73728 + d * 1024, 0, 16);
    }
  }
  if (js != 0) {
    u32x2 v = {pack2(r[2][0], r[2][1]), pack2(r[2][2], r[2][3])};
    __builtin_amdgcn_raw_buffer_store_b64(v, sl, (unsigned)(w * 9216 + 4096 + l * 8) + js * 73728, 0, 16);
  }
  gu32* cnt = (gu32*)(ws + WS_FLAGS + OFF_CNT4 + tile * 256);
  WAIT_V(0);
  if (l == 0) {
    unsigned old = __hip_atomic_fetch_add((LAS unsigned*)(smem + 8 * K4_STAGE), 1u, __ATOMIC_RELAXED, __HIP_MEMORY_SCOPE_WORKGROUP);
    if (old == 7) __hip_atomic_fetch_add(cnt, 1u, RLX_AGENT);
  }
  const int row = T * 128 + (wm * 2 + kk) * 16 + tok;
  const int nfirst = (js == 0) ? 0 : (2 * js + 1), ncnt = (js == 0) ? 3 : 2;
  const float* gbp = (const float*)(ws + WS_MODF) + 2880;
  u32x2 xr[3];
  f32x4 gb[3];
#pragma unroll
  for (int j = 0; j < 3; ++j) {
    if (j < ncnt) {
      const int n0 = cg * 144 + (nfirst + j) * 16 + g * 4;
      xr[j] = *(const u32x2*)(ws + WS_X1 + ((size_t)row * 576 + n0) * 2);
      gb[j] = *(const f32x4*)(gbp + n0);
    }
  }
  if (w == 0) wait_ge(cnt, 4u);
  __builtin_amdgcn_fence(__ATOMIC_ACQUIRE, "wavefront");
  __syncthreads();
  {
    u32x4 pa[4];
    u32x2 pb[4];
#pragma unroll
    for (int sidx = 0; sidx < 4; ++sidx) {
      pa[sidx] = (u32x4){0, 0, 0, 0}; pb[sidx] = (u32x2){0, 0};
      if (sidx != js) {
        pa[sidx] = __builtin_amdgcn_raw_buffer_load_b128(sl, soff + sidx * 73728 + js * 1024, 0, 16);
        if (js == 0) pb[sidx] = __builtin_amdgcn_raw_buffer_load_b64(sl, (unsigned)(w * 9216 + 4096 + l * 8) + sidx * 73728, 0, 16);
      }
    }
#pragma unroll
    for (int n = 0; n < 9; ++n) {
      const int own = (n < 3) ? 0 : ((n - 1) >> 1);
      if (own == js) {
        const int j = n - ((own == 0) ? 0 : (2 * own + 1));
        f32x4 pv[4];
#pragma unroll
        for (int sidx = 0; sidx < 4; ++sidx) {
          const unsigned u0 = (j == 2) ? pb[sidx][0] : pa[sidx][2 * (j & 1)], u1 = (j == 2) ? pb[sidx][1] : pa[sidx][2 * (j & 1) + 1];
          pv[sidx] = (sidx == own) ? r[n] : (f32x4){bf_lo(u0), bf_hi(u0), bf_lo(u1), bf_hi(u1)};
        }
        f32x4 y = (pv[0] + pv[1]) + (pv[2] + pv[3]);
        float xin[4] = {bf_lo(xr[j][0]), bf_hi(xr[j][0]), bf_lo(xr[j][1]), bf_hi(xr[j][1])};
        float o[4];
#pragma unroll
        for (int i = 0; i < 4; ++i) o[i] = xin[i] + rbf(gb[j][i] * rbf(y[i]));
        st8<true>(out + (size_t)row * 576 + cg * 144 + n * 16 + g * 4, (u32x2){pack2(o[0], o[1]), pack2(o[2], o[3])});
      }
    }
  }
}

extern "C" void kernel_launch(void* const* d_in, const int* in_sizes, int n_in,
                              void* d_out, int out_size, void* d_ws, size_t ws_size,
                              hipStream_t stream) {
  const unsigned short* x = (const unsigned short*)d_in[0];
  const unsigned short* wqkv = (const unsigned short*)d_in[1];
  const unsigned short* wphi = (const unsigned short*)d_in[2];
  const unsigned short* wo = (const unsigned short*)d_in[3];
  const unsigned short* wg = (const unsigned short*)d_in[5];
  const unsigned short* wu = (const unsigned short*)d_in[6];
  const unsigned short* wd = (const unsigned short*)d_in[7];
  const unsigned short* c = (const unsigned short*)d_in[8];
  const unsigned short* wmod = (const unsigned short*)d_in[9];
  char* ws = (char*)d_ws;
  (void)hipFuncSetAttribute((const void*)k1_attn, hipFuncAttributeMaxDynamicSharedMemorySize, K1_LDS);
  (void)hipFuncSetAttribute((const void*)k3_gateup, hipFuncAttributeMaxDynamicSharedMemorySize, K3_LDS);
  (void)hipFuncSetAttribute((const void*)k4_down, hipFuncAttributeMaxDynamicSharedMemorySize, K4_LDS);
  k0_prep<<<K0_GRID, 256, 0, stream>>>(x, wqkv, wphi, wo, c, wmod, ws);
  k1_attn<<<256, 512, K1_LDS, stream>>>(x, wd, ws);
  k3_gateup<<<256, 512, K3_LDS, stream>>>(wg, wu, ws);
  k4_down<<<256, 512, K4_LDS, stream>>>(ws, (unsigned short*)d_out);
}
```
